```python
import torch

def _softmax(x, axis=-1):
    return torch.softmax(x, dim=axis)
def _gelu(x):
    return torch.nn.functional.gelu(x, approximate="tanh")
def _silu(x):
    return torch.nn.functional.silu(x)
def _sigmoid(x):
    return torch.sigmoid(x)
def _layernorm(x, axis=-1, eps=1e-5):
    m = x.mean(dim=axis, keepdim=True)
    v = x.var(dim=axis, keepdim=True, unbiased=False)
    return (x - m) / torch.sqrt(v + eps)
def _scatter_add(tgt, idx, src, axis=0):
    return tgt.clone().index_add_(axis, idx, src)
def _segment_sum(src, seg, n):
    out = torch.zeros((n,) + src.shape[1:], dtype=src.dtype)
    return out.index_add_(0, seg, src)
def _pad(x, pw):
    flat = tuple(v for pair in reversed(pw) for v in pair)
    return torch.nn.functional.pad(x, flat)

kernel_name = 'parallel_sum__mha_block__swiglu_ffn__prompt'

def setup_inputs():

    _w = (lambda a: (lambda t: t.to(torch.bfloat16) if t.is_floating_point() else t)(torch.as_tensor(a)))
    def _A_setup():
        B, S, D, H = 1, 2048, 512, 32
        return {
            "x": _w((lambda *s: torch.randn(*s, dtype=torch.bfloat16) * 0.05)(B, S, D)),
            "wqkv": _w((lambda *s: torch.randn(*s, dtype=torch.bfloat16) * 0.2)(D, 3 * D)),
            "wo": _w((lambda *s: torch.randn(*s, dtype=torch.bfloat16) * 0.1)(D, D)),
        }
    _A_in = _A_setup()

    def _B_setup():
        B, S, D, F = 1, 2048, 512, 1376
        return {
            "x": _w((lambda *s: torch.randn(*s, dtype=torch.bfloat16) * 0.05)(B, S, D)),
            "w_gate": _w((lambda *s: torch.randn(*s, dtype=torch.bfloat16) * 0.1)(D, F)),
            "w_up": _w((lambda *s: torch.randn(*s, dtype=torch.bfloat16) * 0.1)(D, F)),
            "w_down": _w((lambda *s: torch.randn(*s, dtype=torch.bfloat16) * 0.1)(F, D)),
        }
    _B_in = _B_setup()

    return {
        "A_x": _A_in["x"],
        "A_wqkv": _A_in["wqkv"],
        "A_wo": _A_in["wo"],
        "B_x": _B_in["x"],
        "B_w_gate": _B_in["w_gate"],
        "B_w_up": _B_in["w_up"],
        "B_w_down": _B_in["w_down"],
    }

def reference(A_x, A_wqkv, A_wo, B_x, B_w_gate, B_w_up, B_w_down):
    def _A_ref(x, wqkv, wo):
        B, S, D = x.shape
        H = 32
        Dh = D // H
        qkv = (lambda x, *d: x.permute(*d))((x @ wqkv).reshape(B, S, 3, H, Dh), 2, 0, 3, 1, 4)
        q, k, v = qkv[0], qkv[1], qkv[2]
        scores = torch.einsum("bhqd,bhkd->bhqk", q, k) * (Dh ** -0.5)
        p = _softmax(scores, axis=-1)
        o = torch.einsum("bhqk,bhkd->bhqd", p, v)
        return (lambda x, *d: x.permute(*d))(o, 0, 2, 1, 3).reshape(B, S, D) @ wo

    def _B_ref(x, w_gate, w_up, w_down):
        g = _silu(x @ w_gate)
        u = x @ w_up
        return (g * u) @ w_down

    x = A_x
    xn = _layernorm(x, axis=-1)
    return x + _A_ref(xn, A_wqkv, A_wo) + _B_ref(xn, B_w_gate, B_w_up, B_w_down)
```

```cpp
#include <hip/hip_runtime.h>
#include <hip/hip_bf16.h>
#include <stdint.h>

typedef unsigned short u16;
typedef __attribute__((ext_vector_type(8))) __bf16 bf16x8;
typedef __attribute__((ext_vector_type(2))) __bf16 bf16x2;
typedef __attribute__((ext_vector_type(2))) float f32x2;
typedef __attribute__((ext_vector_type(16))) float f32x16;
typedef __attribute__((ext_vector_type(4))) unsigned u32x4;
typedef __attribute__((ext_vector_type(2))) unsigned u32x2;
#define DI __device__ __forceinline__
#define MFMA32(a,b,c) __builtin_amdgcn_mfma_f32_32x32x16_bf16((a),(b),(c),0,0,0)

DI float bf2f(u16 b){ return __uint_as_float(((unsigned)b)<<16); }
DI u16 f2bf(float f){ unsigned u=__float_as_uint(f); u += 0x7fffu + ((u>>16)&1u); return (u16)(u>>16); }
DI float rbf(float f){ return bf2f(f2bf(f)); }
DI unsigned pk2(float lo, float hi){ f32x2 v={lo,hi}; bf16x2 b=__builtin_convertvector(v,bf16x2); return __builtin_bit_cast(unsigned,b); }

constexpr int S=2048, D=512, H=32, DH=16, F=1376;
constexpr float QSCALE = 0.25f*1.4426950408889634f;

__global__ void k_ln(const u16* x, u16* xn){
  int row=blockIdx.x; int t=threadIdx.x;
  __shared__ float red[256];
  float a=bf2f(x[row*D+t]), b=bf2f(x[row*D+t+256]);
  red[t]=a+b; __syncthreads();
  for(int s=128;s>0;s>>=1){ if(t<s) red[t]+=red[t+s]; __syncthreads(); }
  float mean=red[0]/D; __syncthreads();
  float da=a-mean, db=b-mean;
  red[t]=da*da+db*db; __syncthreads();
  for(int s=128;s>0;s>>=1){ if(t<s) red[t]+=red[t+s]; __syncthreads(); }
  float var=red[0]/D;
  float m=rbf(mean), v=rbf(var);
  float den=rbf(sqrtf(rbf(v+1e-5f)));
  xn[row*D+t]=f2bf(rbf(a-m)/den);
  xn[row*D+t+256]=f2bf(rbf(b-m)/den);
}
__global__ void k_gemm(const u16* A, const u16* B, float* C, int M, int N, int K){
  int n=blockIdx.x*64+threadIdx.x; int m=blockIdx.y*4+threadIdx.y;
  if(n>=N||m>=M) return;
  float acc=0.f;
  for(int k=0;k<K;k++) acc += bf2f(A[(long)m*K+k])*bf2f(B[(long)k*N+n]);
  C[(long)m*N+n]=acc;
}
__global__ void k_prep(const float* qkv, u16* Qs, u16* Ks, u16* Vt){
  int i=blockIdx.x*256+threadIdx.x;
  int s=i/D, c=i%D; int h=c/DH, d=c%DH;
  float q=qkv[(long)s*3*D+c], k=qkv[(long)s*3*D+D+c], v=qkv[(long)s*3*D+2*D+c];
  { int hh=(d>>2)&1, j=((d>>3)<<2)|(d&3); int tile=s>>5, key=s&31;
    long off=(long)h*S*DH + tile*512 + hh*256 + key*8 + j;
    Qs[off]=f2bf(q*QSCALE); Ks[off]=f2bf(k); }
  { int ks=s>>4, kk=s&15; int hh=(kk>>2)&1, j=((kk>>3)<<2)|(kk&3);
    long off=(long)h*S*DH + ks*256 + hh*128 + d*8 + j;
    Vt[off]=f2bf(v); }
}

DI void glds16(const void* g, unsigned lds_base) {
  unsigned sv; asm volatile("s_mov_b32 %0, m0\n\ts_mov_b32 m0, %2\n\ts_nop 0\n\tglobal_load_lds_dwordx4 %1, off\n\ts_mov_b32 m0, %0" : "=&s"(sv) : "v"(g), "s"(lds_base) : "memory"); }

typedef __attribute__((address_space(3))) const char* lds_cptr;
DI bf16x8 lds_ld(lds_cptr p){ return *(const __attribute__((address_space(3))) bf16x8*)p; }

constexpr int AT_LDS_K=0, AT_LDS_V=65536, AT_LDS_ONE=131072, AT_LDS_BYTES=131072+4096;

DI void pv_step(const f32x16& sc, bf16x8 v0, bf16x8 v1, f32x16& o){
  u32x4 p0, p1;
  #pragma unroll
  for(int i=0;i<4;i++){ p0[i]=pk2(__builtin_amdgcn_exp2f(sc[2*i]),__builtin_amdgcn_exp2f(sc[2*i+1])); }
  #pragma unroll
  for(int i=0;i<4;i++){ p1[i]=pk2(__builtin_amdgcn_exp2f(sc[8+2*i]),__builtin_amdgcn_exp2f(sc[8+2*i+1])); }
  o=MFMA32(v0,__builtin_bit_cast(bf16x8,p0),o);
  o=MFMA32(v1,__builtin_bit_cast(bf16x8,p1),o);
}

__global__ __launch_bounds__(512,2) void k_attn(const u16* __restrict__ Qs, const u16* __restrict__ Ks, const u16* __restrict__ Vt, u16* __restrict__ O){
  extern __shared__ __attribute__((aligned(16))) char lds[];
  const int tid=threadIdx.x, lane=tid&63; const int wid=__builtin_amdgcn_readfirstlane(tid>>6);
  const int b=blockIdx.x; const int h=(b&7)*4+((b>>3)>>3), qb=(b>>3)&7;
  const int r=lane&31, hh=lane>>5;
  const unsigned lds0=(unsigned)(uintptr_t)lds;
  const u16* qp=Qs+(long)h*S*DH+(qb*8+wid)*512+lane*8;
  bf16x8 qf=*(const bf16x8*)qp;
  const u16* kg=Ks+(long)h*S*DH+wid*512+lane*8;
  const u16* vg=Vt+(long)h*S*DH+wid*512+lane*8;
  #pragma unroll
  for(int c=0;c<8;c++){
    glds16(kg+c*4096, lds0+AT_LDS_K+c*8192+wid*1024);
    glds16(vg+c*4096, lds0+AT_LDS_V+c*8192+wid*1024);
  }
  *(u32x2*)(lds+AT_LDS_ONE+tid*8)=(u32x2){0x3F803F80u,0x3F803F80u};
  lds_cptr kp=(lds_cptr)lds+AT_LDS_K+lane*16;
  const bool onel=(r==16);
  lds_cptr vp= onel ? (lds_cptr)lds+AT_LDS_ONE : (lds_cptr)lds+AT_LDS_V+hh*256+(r&15)*16;
  const int vinc= onel?0:4096;
  asm volatile("s_waitcnt vmcnt(14) lgkmcnt(0)\n\ts_barrier":::"memory");
  f32x16 negm; f32x16 sc;
  {
    f32x16 z={}; bf16x8 k0=lds_ld(kp);
    f32x16 s0=MFMA32(k0,qf,z);
    float m=s0[0];
    #pragma unroll
    for(int i=1;i<16;i++) m=fmaxf(m,s0[i]);
    auto rr=__builtin_amdgcn_permlane32_swap(__float_as_uint(m),__float_as_uint(m),false,false);
    m=fmaxf(__uint_as_float(rr[0]),__uint_as_float(rr[1]));
    float nm=-(m+60.f);
    #pragma unroll
    for(int i=0;i<16;i++){ negm[i]=nm; sc[i]=s0[i]+nm; }
  }
  f32x16 o={};
  #pragma unroll 1
  for(int it=0; it<16; it++){
    if(it==1){ asm volatile("s_waitcnt vmcnt(0)\n\ts_barrier":::"memory"); }
    #pragma unroll
    for(int tt=0;tt<4;tt++){
      bf16x8 kn=lds_ld(kp+(tt+1)*1024);
      bf16x8 v0=lds_ld(vp+tt*1024), v1=lds_ld(vp+tt*1024+512);
      f32x16 sn=MFMA32(kn,qf,negm);
      pv_step(sc,v0,v1,o);
      sc=sn;
    }
    kp+=4096; vp+=vinc;
  }
  float l=o[8];
  { auto rr=__builtin_amdgcn_permlane32_swap(__float_as_uint(l),__float_as_uint(l),false,false); l=__uint_as_float(rr[0]); }
  bool bad = !(l<1e30f);
  #pragma unroll
  for(int i=0;i<8;i++) bad = bad || !(fabsf(o[i])<1e37f);
  if(__builtin_expect(__any(bad),0)){
    const u16* kgl=Ks+(long)h*S*DH+lane*8;
    const u16* vgl=Vt+(long)h*S*DH+hh*128+(r&15)*8;
    float m=-INFINITY; f32x16 z={};
    #pragma unroll
    for(int i=0;i<16;i++) o[i]=0.f;
    for(int t=0;t<64;t++){
      bf16x8 kf=*(const bf16x8*)(kgl+t*512);
      bf16x8 v0=*(const bf16x8*)(vgl+t*512), v1=*(const bf16x8*)(vgl+t*512+256);
      if(onel){ u32x4 one={0x3F803F80u,0x3F803F80u,0x3F803F80u,0x3F803F80u}; v0=__builtin_bit_cast(bf16x8,one); v1=v0; }
      f32x16 s0=MFMA32(kf,qf,z);
      float tm=s0[0];
      #pragma unroll
      for(int i=1;i<16;i++) tm=fmaxf(tm,s0[i]);
      auto rr=__builtin_amdgcn_permlane32_swap(__float_as_uint(tm),__float_as_uint(tm),false,false);
      tm=fmaxf(__uint_as_float(rr[0]),__uint_as_float(rr[1]));
      float mn=fmaxf(m,tm);
      float alpha=__builtin_amdgcn_exp2f(m-mn); m=mn;
      #pragma unroll
      for(int i=0;i<16;i++){ o[i]*=alpha; s0[i]-=mn; }
      pv_step(s0,v0,v1,o);
    }
    l=o[8];
    auto rr=__builtin_amdgcn_permlane32_swap(__float_as_uint(l),__float_as_uint(l),false,false); l=__uint_as_float(rr[0]);
  }
  float rl=1.0f/l;
  u32x2 w0={pk2(o[0]*rl,o[1]*rl),pk2(o[2]*rl,o[3]*rl)};
  u32x2 w1={pk2(o[4]*rl,o[5]*rl),pk2(o[6]*rl,o[7]*rl)};
  u16* op=O+(long)((qb*8+wid)*32+r)*D+h*DH+hh*4;
  *(u32x2*)op=w0; *(u32x2*)(op+8)=w1;
}

__global__ void k_swiglu(const float* g, const float* u, u16* hh, int n){
  int i=blockIdx.x*256+threadIdx.x; if(i>=n) return;
  float gg=g[i]; float s=gg/(1.f+expf(-gg)); hh[i]=f2bf(s*u[i]);
}
__global__ void k_final(const u16* x, const float* a, const float* b, u16* out, int n){
  int i=blockIdx.x*256+threadIdx.x; if(i>=n) return;
  out[i]=f2bf(bf2f(x[i])+a[i]+b[i]);
}
extern "C" void kernel_launch(void* const* d_in, const int* in_sizes, int n_in, void* d_out, int out_size, void* d_ws, size_t ws_size, hipStream_t stream){
  const u16* x=(const u16*)d_in[0]; const u16* wqkv=(const u16*)d_in[1]; const u16* wo=(const u16*)d_in[2];
  const u16* wg=(const u16*)d_in[4]; const u16* wu=(const u16*)d_in[5]; const u16* wd=(const u16*)d_in[6];
  char* ws=(char*)d_ws;
  u16* xn=(u16*)ws; ws+=S*D*2;
  float* qkv=(float*)ws; ws+=(size_t)S*3*D*4;
  float* g=(float*)ws; ws+=(size_t)S*F*4;
  float* u=(float*)ws; ws+=(size_t)S*F*4;
  u16* hh=(u16*)ws; ws+=(size_t)S*F*2;
  u16* o=(u16*)ws; ws+=(size_t)S*D*2;
  float* a=(float*)ws; ws+=(size_t)S*D*4;
  float* b=(float*)ws; ws+=(size_t)S*D*4;
  u16* Qs=(u16*)ws; ws+=(size_t)S*D*2;
  u16* Ks=(u16*)ws; ws+=(size_t)S*D*2;
  u16* Vt=(u16*)ws; ws+=(size_t)S*D*2;
  hipFuncSetAttribute((const void*)k_attn, hipFuncAttributeMaxDynamicSharedMemorySize, AT_LDS_BYTES);
  k_ln<<<S,256,0,stream>>>(x,xn);
  k_gemm<<<dim3(3*D/64,S/4),dim3(64,4),0,stream>>>(xn,wqkv,qkv,S,3*D,D);
  k_gemm<<<dim3((F+63)/64,S/4),dim3(64,4),0,stream>>>(xn,wg,g,S,F,D);
  k_gemm<<<dim3((F+63)/64,S/4),dim3(64,4),0,stream>>>(xn,wu,u,S,F,D);
  k_swiglu<<<(S*F+255)/256,256,0,stream>>>(g,u,hh,S*F);
  k_prep<<<S*D/256,256,0,stream>>>(qkv,Qs,Ks,Vt);
  k_attn<<<256,512,AT_LDS_BYTES,stream>>>(Qs,Ks,Vt,o);
  k_gemm<<<dim3(D/64,S/4),dim3(64,4),0,stream>>>(o,wo,a,S,D,D);
  k_gemm<<<dim3(D/64,S/4),dim3(64,4),0,stream>>>(hh,wd,b,S,D,F);
  k_final<<<(S*D+255)/256,256,0,stream>>>(x,a,b,(u16*)d_out,S*D);
}
```

```cpp
#define BARMODE 0
#define FIRSTW "0"
#include <hip/hip_runtime.h>
#include <hip/hip_bf16.h>
#include <stdint.h>

typedef unsigned short u16;
typedef __attribute__((ext_vector_type(8))) __bf16 bf16x8;
typedef __attribute__((ext_vector_type(2))) __bf16 bf16x2;
typedef __attribute__((ext_vector_type(2))) float f32x2;
typedef __attribute__((ext_vector_type(16))) float f32x16;
typedef __attribute__((ext_vector_type(4))) unsigned u32x4;
typedef __attribute__((ext_vector_type(2))) unsigned u32x2;
#define DI __device__ __forceinline__
#define MFMA32(a,b,c) __builtin_amdgcn_mfma_f32_32x32x16_bf16((a),(b),(c),0,0,0)

DI float bf2f(u16 b){ return __uint_as_float(((unsigned)b)<<16); }
DI u16 f2bf(float f){ unsigned u=__float_as_uint(f); u += 0x7fffu + ((u>>16)&1u); return (u16)(u>>16); }
DI float rbf(float f){ return bf2f(f2bf(f)); }
DI unsigned pk2(float lo, float hi){ f32x2 v={lo,hi}; bf16x2 b=__builtin_convertvector(v,bf16x2); return __builtin_bit_cast(unsigned,b); }

constexpr int S=2048, D=512, H=32, DH=16, F=1376;
constexpr float QSCALE = 0.25f*1.4426950408889634f;

__global__ void k_ln(const u16* x, u16* xn){
  int row=blockIdx.x; int t=threadIdx.x;
  __shared__ float red[256];
  float a=bf2f(x[row*D+t]), b=bf2f(x[row*D+t+256]);
  red[t]=a+b; __syncthreads();
  for(int s=128;s>0;s>>=1){ if(t<s) red[t]+=red[t+s]; __syncthreads(); }
  float mean=red[0]/D; __syncthreads();
  float da=a-mean, db=b-mean;
  red[t]=da*da+db*db; __syncthreads();
  for(int s=128;s>0;s>>=1){ if(t<s) red[t]+=red[t+s]; __syncthreads(); }
  float var=red[0]/D;
  float m=rbf(mean), v=rbf(var);
  float den=rbf(sqrtf(rbf(v+1e-5f)));
  xn[row*D+t]=f2bf(rbf(a-m)/den);
  xn[row*D+t+256]=f2bf(rbf(b-m)/den);
}
__global__ void k_gemm(const u16* A, const u16* B, float* C, int M, int N, int K){
  int n=blockIdx.x*64+threadIdx.x; int m=blockIdx.y*4+threadIdx.y;
  if(n>=N||m>=M) return;
  float acc=0.f;
  for(int k=0;k<K;k++) acc += bf2f(A[(long)m*K+k])*bf2f(B[(long)k*N+n]);
  C[(long)m*N+n]=acc;
}
__global__ void k_prep(const float* qkv, u16* Qs, u16* Ks, u16* Vt){
  int i=blockIdx.x*256+threadIdx.x;
  int s=i/D, c=i%D; int h=c/DH, d=c%DH;
  float q=qkv[(long)s*3*D+c], k=qkv[(long)s*3*D+D+c], v=qkv[(long)s*3*D+2*D+c];
  { int hh=(d>>2)&1, j=((d>>3)<<2)|(d&3); int tile=s>>5, key=s&31;
    long off=(long)h*S*DH + tile*512 + hh*256 + key*8 + j;
    Qs[off]=f2bf(q*QSCALE); Ks[off]=f2bf(k); }
  { int ks=s>>4, kk=s&15; int hh=(kk>>2)&1, j=((kk>>3)<<2)|(kk&3);
    long off=(long)h*S*DH + ks*256 + hh*128 + d*8 + j;
    Vt[off]=f2bf(v); }
}

DI void glds16(const void* g, unsigned lds_base) {
  unsigned sv; asm volatile("s_mov_b32 %0, m0\n\ts_mov_b32 m0, %2\n\ts_nop 0\n\tglobal_load_lds_dwordx4 %1, off\n\ts_mov_b32 m0, %0" : "=&s"(sv) : "v"(g), "s"(lds_base) : "memory"); }

typedef __attribute__((address_space(3))) const char* lds_cptr;
DI bf16x8 lds_ld(lds_cptr p){ return *(const __attribute__((address_space(3))) bf16x8*)p; }

typedef __attribute__((ext_vector_type(4))) float f32x4;
typedef __attribute__((ext_vector_type(4))) short s16x4;
typedef short v4i16_t __attribute__((ext_vector_type(4)));
#define MFMA16(a,b,c) __builtin_amdgcn_mfma_f32_16x16x32_bf16((a),(b),(c),0,0,0)
DI s16x4 lds_tr(lds_cptr p){ return __builtin_bit_cast(s16x4, __builtin_amdgcn_ds_read_tr16_b64_v4i16((__attribute__((address_space(3))) v4i16_t*)p)); }
DI bf16x8 cat8(s16x4 a, s16x4 b){ return __builtin_bit_cast(bf16x8, __builtin_shufflevector(a,b,0,1,2,3,4,5,6,7)); }
DI float dpp_add(float v, const int ctrl_sel){
  int t;
  if(ctrl_sel==0) t=__builtin_amdgcn_mov_dpp(__float_as_int(v),0xB1,0xF,0xF,true);
  else if(ctrl_sel==1) t=__builtin_amdgcn_mov_dpp(__float_as_int(v),0x4E,0xF,0xF,true);
  else t=__builtin_amdgcn_mov_dpp(__float_as_int(v),0x141,0xF,0xF,true);
  return v+__int_as_float(t);
}
DI float lo16f(unsigned u){ return __uint_as_float(u<<16); }
DI float hi16f(unsigned u){ return __uint_as_float(u&0xffff0000u); }
DI float dot2bf(unsigned a, unsigned b, float c){
  return __builtin_amdgcn_fdot2_f32_bf16(__builtin_bit_cast(bf16x2,a),__builtin_bit_cast(bf16x2,b),c,false); }

constexpr int K1_WST=24576, K1_NST=6, K1_X=K1_WST*K1_NST, K1_LDS=K1_X+16384;

__global__ __launch_bounds__(512,2) void k1_qkv(const u16* __restrict__ x, const u16* __restrict__ wqkv,
     u16* __restrict__ Qs, u16* __restrict__ Ks, u16* __restrict__ Vt, u16* __restrict__ xn){
  extern __shared__ __attribute__((aligned(16))) char lds[];
  const int tid=threadIdx.x, lane=tid&63; const int wid=__builtin_amdgcn_readfirstlane(tid>>6);
  const int b=blockIdx.x; const int rb=(b&7)*4+((b>>3)&3), hg=b>>5;
  const unsigned lds0=(unsigned)(uintptr_t)lds;
  const int rr=lane>>3, p=lane&7;
  const int lrow=wid*8+rr;
  const u16* xp=x+(long)(rb*64+lrow)*D+p*8;
  u32x4 xv[8];
  #pragma unroll
  for(int i=0;i<8;i++) asm volatile("global_load_dwordx4 %0, %1, off offset:%2" : "=v"(xv[i]) : "v"(xp), "n"(i*128) : "memory");
  {
    const int k=wid*8+rr; const int f=((k>>1)&1)|(((k>>3)&1)<<1);
    const int ct=(p>>1)^f;
    const u16* wp=wqkv+(long)k*(3*D)+hg*64+ct*16+(p&1)*8;
    #pragma unroll
    for(int st=0;st<K1_NST;st++){
      #pragma unroll
      for(int ty=0;ty<3;ty++) glds16(wp+(long)st*64*(3*D)+ty*D, lds0+st*K1_WST+ty*8192+wid*1024);
    }
  }
  asm volatile("s_waitcnt vmcnt(18)" : "+v"(xv[0]),"+v"(xv[1]),"+v"(xv[2]),"+v"(xv[3]),"+v"(xv[4]),"+v"(xv[5]),"+v"(xv[6]),"+v"(xv[7]) :: "memory");
  float s1=0.f, s2=0.f;
  #pragma unroll
  for(int i=0;i<8;i++){
    #pragma unroll
    for(int j=0;j<4;j++){ s1=dot2bf(xv[i][j],0x3F803F80u,s1); s2=dot2bf(xv[i][j],xv[i][j],s2); }
  }
  s1=dpp_add(s1,0); s2=dpp_add(s2,0); s1=dpp_add(s1,1); s2=dpp_add(s2,1); s1=dpp_add(s1,2); s2=dpp_add(s2,2);
  const float mean=s1*(1.0f/D);
  float var=s2*(1.0f/D)-mean*mean;
  if(__builtin_expect(__any(!(mean*mean<=16.f*var)),0)){
    float q2=0.f;
    #pragma unroll
    for(int i=0;i<8;i++){
      #pragma unroll
      for(int j=0;j<4;j++){ float a=lo16f(xv[i][j])-mean, c=hi16f(xv[i][j])-mean; q2=fmaf(a,a,q2); q2=fmaf(c,c,q2); }
    }
    q2=dpp_add(q2,0); q2=dpp_add(q2,1); q2=dpp_add(q2,2);
    var=q2*(1.0f/D);
  }
  const float mb=rbf(mean);
  const float den=rbf(sqrtf(rbf(rbf(var)+1e-5f)));
  const float rinv=1.0f/den;
  char* xw=lds+K1_X+lrow*128+((p^((lrow>>1)&7))*16);
#define NORM(i) do{ u32x4 o_; _Pragma("unroll") for(int j=0;j<4;j++){ unsigned c_=pk2(lo16f(xv[i][j])-mb, hi16f(xv[i][j])-mb); o_[j]=pk2(lo16f(c_)*rinv, hi16f(c_)*rinv); } \
     xv[i]=o_; *(u32x4*)(xw+((i)&1)*8192)=o_; }while(0)
  NORM(0);
  const int rt2=wid&1, hd=wid>>1;
  const int g=lane>>4, r16=lane&15;
  lds_cptr xa[2];
  #pragma unroll
  for(int tt=0;tt<2;tt++){ const int row=rt2*32+tt*16+r16; xa[tt]=(lds_cptr)lds+K1_X+row*128; }
  const int xsw=(r16>>1)&7;
  const int xo0=((g)^xsw)*16, xo1=((4+g)^xsw)*16;
  const int q4=(lane&15)>>2, p4=lane&3;
  const int fk=((q4>>1)&1)|((g&1)<<1);
  lds_cptr wa=(lds_cptr)lds+(8*g+q4)*128+((hd^fk)*32)+p4*8;
  f32x4 aq[2]={}, ak[2]={}, av[2]={};
  const u16* wp2=wqkv+(long)(wid*8+rr)*(3*D)+hg*64+(((p>>1)^((((wid*8+rr)>>1)&1)|((((wid*8+rr)>>3)&1)<<1)))*16)+(p&1)*8;
  #pragma unroll
  for(int c=0;c<8;c++){
    constexpr int NW[8]={15,12,12,12,9,6,3,0};
    asm volatile("s_waitcnt vmcnt(%0) lgkmcnt(0)\n\ts_barrier"::"n"(NW[c]):"memory");
    if(c==1||c==2){
      #pragma unroll
      for(int ty=0;ty<3;ty++) glds16(wp2+(long)(c+5)*64*(3*D)+ty*D, lds0+(c-1)*K1_WST+ty*8192+wid*1024);
    }
    const int slot=(c%K1_NST)*K1_WST;
    #pragma unroll
    for(int s=0;s<2;s++){
      bf16x8 x0=lds_ld(xa[0]+(c&1)*8192+(s?xo1:xo0));
      bf16x8 x1=lds_ld(xa[1]+(c&1)*8192+(s?xo1:xo0));
      bf16x8 wq=cat8(lds_tr(wa+slot+s*4096), lds_tr(wa+slot+s*4096+512));
      bf16x8 wk=cat8(lds_tr(wa+slot+8192+s*4096), lds_tr(wa+slot+8192+s*4096+512));
      bf16x8 wv=cat8(lds_tr(wa+slot+16384+s*4096), lds_tr(wa+slot+16384+s*4096+512));
      aq[0]=MFMA16(wq,x0,aq[0]); aq[1]=MFMA16(wq,x1,aq[1]);
      ak[0]=MFMA16(wk,x0,ak[0]); ak[1]=MFMA16(wk,x1,ak[1]);
      av[0]=MFMA16(x0,wv,av[0]); av[1]=MFMA16(x1,wv,av[1]);
    }
    if(c<7){ NORM((c+1)&7); }
  }
  const int h=hg*4+hd; const int T=rb*2+rt2;
  #pragma unroll
  for(int tt=0;tt<2;tt++){
    const long qo=(long)h*S*DH+T*512+(g>>1)*256+(tt*16+r16)*8+(g&1)*4;
    u32x2 wq={pk2(aq[tt][0]*QSCALE,aq[tt][1]*QSCALE),pk2(aq[tt][2]*QSCALE,aq[tt][3]*QSCALE)};
    u32x2 wk={pk2(ak[tt][0],ak[tt][1]),pk2(ak[tt][2],ak[tt][3])};
    *(u32x2*)(Qs+qo)=wq; *(u32x2*)(Ks+qo)=wk;
    const long vo=(long)h*S*DH+(2*T+tt)*256+(g&1)*128+r16*8+(g>>1)*4;
    u32x2 wv={pk2(av[tt][0],av[tt][1]),pk2(av[tt][2],av[tt][3])};
    *(u32x2*)(Vt+vo)=wv;
  }
  {
    u32x4 v=xv[0];
    #pragma unroll
    for(int i=1;i<8;i++) if(hg==i) v=xv[i];
    *(u32x4*)(xn+(long)(rb*64+lrow)*D+hg*64+p*8)=v;
  }
}

constexpr int AT_THREADS=1024; constexpr int AT_LDS_K=0, AT_LDS_V=65536, AT_LDS_ONE=131072, AT_LDS_BYTES=131072+4096;

DI void pv_step(const f32x16& sc, bf16x8 v0, bf16x8 v1, f32x16& o){
  u32x4 p0, p1;
  #pragma unroll
  for(int i=0;i<4;i++){ p0[i]=pk2(__builtin_amdgcn_exp2f(sc[2*i]),__builtin_amdgcn_exp2f(sc[2*i+1])); }
  #pragma unroll
  for(int i=0;i<4;i++){ p1[i]=pk2(__builtin_amdgcn_exp2f(sc[8+2*i]),__builtin_amdgcn_exp2f(sc[8+2*i+1])); }
  o=MFMA32(v0,__builtin_bit_cast(bf16x8,p0),o);
  o=MFMA32(v1,__builtin_bit_cast(bf16x8,p1),o);
}

#define SB() __builtin_amdgcn_sched_barrier(0)
#define EX(x) __builtin_amdgcn_exp2f(x)
#define STAMP(i)
__global__ __launch_bounds__(1024,4) void k_attn(const u16* __restrict__ Qs, const u16* __restrict__ Ks, const u16* __restrict__ Vt, u16* __restrict__ O){
  extern __shared__ __attribute__((aligned(16))) char lds[];
  const int tid=threadIdx.x, lane=tid&63; const int wid=__builtin_amdgcn_readfirstlane(tid>>6);
  const int b=blockIdx.x; const int h=(b&7)*4+((b>>3)>>3), qb=(b>>3)&7;
  const int r=lane&31, hh=lane>>5;
  const int qt=wid&7, kh=wid>>3;
  const unsigned lds0=(unsigned)(uintptr_t)lds;
  STAMP(0);
  const u16* qp=Qs+(long)h*S*DH+(qb*8+qt)*512+lane*8;
  bf16x8 qf;
  asm volatile("global_load_dwordx4 %0, %1, off" : "=v"(qf) : "v"(qp) : "memory");
  { const u16* g0=((qt>>2)?Vt:Ks)+(long)h*S*DH+(kh*32+(qt&3))*512+lane*8;
    const unsigned l0=lds0+((qt>>2)?AT_LDS_V:AT_LDS_K)+(kh*32+(qt&3))*1024;
    #pragma unroll
    for(int c=0;c<8;c++) glds16(g0+c*2048, l0+c*4096);
  }
  { unsigned v=((tid&63)<4)?0x3F803F80u:0u; *(unsigned*)(lds+AT_LDS_ONE+tid*4)=v; }
  lds_cptr kp0=(lds_cptr)lds+AT_LDS_K+lane*16;
  lds_cptr kp=kp0+kh*32768;
  const bool cl=(r>=16);
  lds_cptr vp= (lds_cptr)lds+(cl ? AT_LDS_ONE : AT_LDS_V+kh*32768)+hh*256+(r&15)*16;
  const int vinc= cl?0:2048;
  asm volatile("s_waitcnt vmcnt(" FIRSTW ") lgkmcnt(0)\n\ts_barrier": "+v"(qf) ::"memory");
  STAMP(1);
  f32x16 negm; f32x16 sA, sB;
  bf16x8 kA, kB, vA0, vA1, vB0, vB1;
  {
    f32x16 z={}; bf16x8 k0=lds_ld(kp0); bf16x8 k1=lds_ld(kp);
    kA=lds_ld(kp+1024); vA0=lds_ld(vp); vA1=lds_ld(vp+512);
    f32x16 s0=MFMA32(k0,qf,z);
    float m=s0[0];
    #pragma unroll
    for(int i=1;i<16;i++) m=fmaxf(m,s0[i]);
    auto rr=__builtin_amdgcn_permlane32_swap(__float_as_uint(m),__float_as_uint(m),false,false);
    m=fmaxf(__uint_as_float(rr[0]),__uint_as_float(rr[1]));
    float nm=-(m+60.f);
    #pragma unroll
    for(int i=0;i<16;i++){ negm[i]=nm; }
    sA=MFMA32(k1,qf,negm);
  }
  f32x16 o={};
#define STEP(sC,sN,kU,kL,vU0,vU1,vL0,vL1,KOFF,VOFF) do{ \
    u32x4 p0,p1; \
    kL=lds_ld(kp+(KOFF)); vL0=lds_ld(vp+(VOFF)); vL1=lds_ld(vp+(VOFF)+512); \
    float e0=EX(sC[0]),e1=EX(sC[1]),e2=EX(sC[2]),e3=EX(sC[3]); SB(); \
    sN=MFMA32(kU,qf,negm); SB(); \
    float e4=EX(sC[4]),e5=EX(sC[5]),e6=EX(sC[6]),e7=EX(sC[7]); \
    p0[0]=pk2(e0,e1); p0[1]=pk2(e2,e3); p0[2]=pk2(e4,e5); p0[3]=pk2(e6,e7); SB(); \
    float e8=EX(sC[8]),e9=EX(sC[9]); SB(); \
    o=MFMA32(vU0,__builtin_bit_cast(bf16x8,p0),o); SB(); \
    float e10=EX(sC[10]),e11=EX(sC[11]),e12=EX(sC[12]),e13=EX(sC[13]),e14=EX(sC[14]),e15=EX(sC[15]); \
    p1[0]=pk2(e8,e9); p1[1]=pk2(e10,e11); p1[2]=pk2(e12,e13); p1[3]=pk2(e14,e15); SB(); \
    o=MFMA32(vU1,__builtin_bit_cast(bf16x8,p1),o); SB(); \
  }while(0)
#define ITER() do{ STEP(sA,sB,kA,kB,vA0,vA1,vB0,vB1,2048,1024); STEP(sB,sA,kB,kA,vB0,vB1,vA0,vA1,3072,2048); kp+=2048; vp+=vinc; }while(0)
  #pragma unroll 1
  for(int it=0;it<16;it++){ ITER(); }
  STAMP(2);
  __syncthreads();
  float* xch=(float*)lds + qt*64*9 + lane;
  if(kh==1){
    #pragma unroll
    for(int i=0;i<9;i++) xch[i*64]=o[i];
  }
  __syncthreads();
  if(kh==1) return;
  #pragma unroll
  for(int i=0;i<9;i++) o[i]+=xch[i*64];
  float l=o[8];
  { auto rr=__builtin_amdgcn_permlane32_swap(__float_as_uint(l),__float_as_uint(l),false,false); l=__uint_as_float(rr[0]); }
  bool bad = !(l<1e30f);
  #pragma unroll
  for(int i=0;i<8;i++) bad = bad || !(fabsf(o[i])<1e37f);
  if(__builtin_expect(__any(bad),0)){
    const u16* kgl=Ks+(long)h*S*DH+lane*8;
    const u16* vgl=Vt+(long)h*S*DH+hh*128+(r&15)*8;
    float m=-INFINITY; f32x16 z={};
    #pragma unroll
    for(int i=0;i<16;i++) o[i]=0.f;
    for(int t=0;t<64;t++){
      bf16x8 kf=*(const bf16x8*)(kgl+t*512);
      bf16x8 v0=*(const bf16x8*)(vgl+t*512), v1=*(const bf16x8*)(vgl+t*512+256);
      if(cl){ u32x4 one={0x3F803F80u,0x3F803F80u,0x3F803F80u,0x3F803F80u}; v0=__builtin_bit_cast(bf16x8,one); v1=v0; }
      f32x16 s0=MFMA32(kf,qf,z);
      float tm=s0[0];
      #pragma unroll
      for(int i=1;i<16;i++) tm=fmaxf(tm,s0[i]);
      auto rr=__builtin_amdgcn_permlane32_swap(__float_as_uint(tm),__float_as_uint(tm),false,false);
      tm=fmaxf(__uint_as_float(rr[0]),__uint_as_float(rr[1]));
      float mn=fmaxf(m,tm);
      float alpha=__builtin_amdgcn_exp2f(m-mn); m=mn;
      #pragma unroll
      for(int i=0;i<16;i++){ o[i]*=alpha; s0[i]-=mn; }
      pv_step(s0,v0,v1,o);
    }
    l=o[8];
    auto rr=__builtin_amdgcn_permlane32_swap(__float_as_uint(l),__float_as_uint(l),false,false); l=__uint_as_float(rr[0]);
  }
  float rl=1.0f/l;
  u32x2 w0={pk2(o[0]*rl,o[1]*rl),pk2(o[2]*rl,o[3]*rl)};
  u32x2 w1={pk2(o[4]*rl,o[5]*rl),pk2(o[6]*rl,o[7]*rl)};
  u16* op=O+(long)((qb*8+qt)*32+r)*D+h*DH+hh*4;
  *(u32x2*)op=w0; *(u32x2*)(op+8)=w1;
  STAMP(3);
}

__global__ void k_swiglu(const float* g, const float* u, u16* hh, int n){
  int i=blockIdx.x*256+threadIdx.x; if(i>=n) return;
  float gg=g[i]; float s=gg/(1.f+expf(-gg)); hh[i]=f2bf(s*u[i]);
}
__global__ void k_final(const u16* x, const float* a, const float* b, u16* out, int n){
  int i=blockIdx.x*256+threadIdx.x; if(i>=n) return;
  out[i]=f2bf(bf2f(x[i])+a[i]+b[i]);
}
extern "C" void kernel_launch(void* const* d_in, const int* in_sizes, int n_in, void* d_out, int out_size, void* d_ws, size_t ws_size, hipStream_t stream){
  const u16* x=(const u16*)d_in[0]; const u16* wqkv=(const u16*)d_in[1]; const u16* wo=(const u16*)d_in[2];
  const u16* wg=(const u16*)d_in[4]; const u16* wu=(const u16*)d_in[5]; const u16* wd=(const u16*)d_in[6];
  char* ws=(char*)d_ws;
  u16* xn=(u16*)ws; ws+=S*D*2;
  float* qkv=(float*)ws; ws+=(size_t)S*3*D*4;
  float* g=(float*)ws; ws+=(size_t)S*F*4;
  float* u=(float*)ws; ws+=(size_t)S*F*4;
  u16* hh=(u16*)ws; ws+=(size_t)S*F*2;
  u16* o=(u16*)ws; ws+=(size_t)S*D*2;
  float* a=(float*)ws; ws+=(size_t)S*D*4;
  float* b=(float*)ws; ws+=(size_t)S*D*4;
  u16* Qs=(u16*)ws; ws+=(size_t)S*D*2;
  u16* Ks=(u16*)ws; ws+=(size_t)S*D*2;
  u16* Vt=(u16*)ws; ws+=(size_t)S*D*2;
  hipFuncSetAttribute((const void*)k_attn, hipFuncAttributeMaxDynamicSharedMemorySize, AT_LDS_BYTES);
  hipFuncSetAttribute((const void*)k1_qkv, hipFuncAttributeMaxDynamicSharedMemorySize, K1_LDS);
  k1_qkv<<<256,512,K1_LDS,stream>>>(x,wqkv,Qs,Ks,Vt,xn);
  k_gemm<<<dim3((F+63)/64,S/4),dim3(64,4),0,stream>>>(xn,wg,g,S,F,D);
  k_gemm<<<dim3((F+63)/64,S/4),dim3(64,4),0,stream>>>(xn,wu,u,S,F,D);
  k_swiglu<<<(S*F+255)/256,256,0,stream>>>(g,u,hh,S*F);
  k_attn<<<256,1024,AT_LDS_BYTES,stream>>>(Qs,Ks,Vt,o);
  k_gemm<<<dim3(D/64,S/4),dim3(64,4),0,stream>>>(o,wo,a,S,D,D);
  k_gemm<<<dim3(D/64,S/4),dim3(64,4),0,stream>>>(hh,wd,b,S,D,F);
  k_final<<<(S*D+255)/256,256,0,stream>>>(x,a,b,(u16*)d_out,S*D);
}
```

```cpp
#define BARMODE 0
#define FIRSTW "0"
#include <hip/hip_runtime.h>
#include <hip/hip_bf16.h>
#include <stdint.h>

typedef unsigned short u16;
typedef __attribute__((ext_vector_type(8))) __bf16 bf16x8;
typedef __attribute__((ext_vector_type(2))) __bf16 bf16x2;
typedef __attribute__((ext_vector_type(2))) float f32x2;
typedef __attribute__((ext_vector_type(16))) float f32x16;
typedef __attribute__((ext_vector_type(4))) unsigned u32x4;
typedef __attribute__((ext_vector_type(2))) unsigned u32x2;
#define DI __device__ __forceinline__
#define MFMA32(a,b,c) __builtin_amdgcn_mfma_f32_32x32x16_bf16((a),(b),(c),0,0,0)

DI float bf2f(u16 b){ return __uint_as_float(((unsigned)b)<<16); }
DI u16 f2bf(float f){ unsigned u=__float_as_uint(f); u += 0x7fffu + ((u>>16)&1u); return (u16)(u>>16); }
DI float rbf(float f){ return bf2f(f2bf(f)); }
DI unsigned pk2(float lo, float hi){ f32x2 v={lo,hi}; bf16x2 b=__builtin_convertvector(v,bf16x2); return __builtin_bit_cast(unsigned,b); }

constexpr int S=2048, D=512, H=32, DH=16, F=1376;
constexpr float QSCALE = 0.25f*1.4426950408889634f;

__global__ void k_ln(const u16* x, u16* xn){
  int row=blockIdx.x; int t=threadIdx.x;
  __shared__ float red[256];
  float a=bf2f(x[row*D+t]), b=bf2f(x[row*D+t+256]);
  red[t]=a+b; __syncthreads();
  for(int s=128;s>0;s>>=1){ if(t<s) red[t]+=red[t+s]; __syncthreads(); }
  float mean=red[0]/D; __syncthreads();
  float da=a-mean, db=b-mean;
  red[t]=da*da+db*db; __syncthreads();
  for(int s=128;s>0;s>>=1){ if(t<s) red[t]+=red[t+s]; __syncthreads(); }
  float var=red[0]/D;
  float m=rbf(mean), v=rbf(var);
  float den=rbf(sqrtf(rbf(v+1e-5f)));
  xn[row*D+t]=f2bf(rbf(a-m)/den);
  xn[row*D+t+256]=f2bf(rbf(b-m)/den);
}
__global__ void k_gemm(const u16* A, const u16* B, float* C, int M, int N, int K){
  int n=blockIdx.x*64+threadIdx.x; int m=blockIdx.y*4+threadIdx.y;
  if(n>=N||m>=M) return;
  float acc=0.f;
  for(int k=0;k<K;k++) acc += bf2f(A[(long)m*K+k])*bf2f(B[(long)k*N+n]);
  C[(long)m*N+n]=acc;
}
__global__ void k_prep(const float* qkv, u16* Qs, u16* Ks, u16* Vt){
  int i=blockIdx.x*256+threadIdx.x;
  int s=i/D, c=i%D; int h=c/DH, d=c%DH;
  float q=qkv[(long)s*3*D+c], k=qkv[(long)s*3*D+D+c], v=qkv[(long)s*3*D+2*D+c];
  { int hh=(d>>2)&1, j=((d>>3)<<2)|(d&3); int tile=s>>5, key=s&31;
    long off=(long)h*S*DH + tile*512 + hh*256 + key*8 + j;
    Qs[off]=f2bf(q*QSCALE); Ks[off]=f2bf(k); }
  { int ks=s>>4, kk=s&15; int hh=(kk>>2)&1, j=((kk>>3)<<2)|(kk&3);
    long off=(long)h*S*DH + ks*256 + hh*128 + d*8 + j;
    Vt[off]=f2bf(v); }
}

DI void glds16(const void* g, unsigned lds_base) {
  unsigned sv; asm volatile("s_mov_b32 %0, m0\n\ts_mov_b32 m0, %2\n\ts_nop 0\n\tglobal_load_lds_dwordx4 %1, off\n\ts_mov_b32 m0, %0" : "=&s"(sv) : "v"(g), "s"(lds_base) : "memory"); }

typedef __attribute__((address_space(3))) const char* lds_cptr;
DI bf16x8 lds_ld(lds_cptr p){ return *(const __attribute__((address_space(3))) bf16x8*)p; }

#define SB() __builtin_amdgcn_sched_barrier(0)
typedef __attribute__((ext_vector_type(4))) float f32x4;
typedef __attribute__((ext_vector_type(4))) short s16x4;
typedef short v4i16_t __attribute__((ext_vector_type(4)));
#define MFMA16(a,b,c) __builtin_amdgcn_mfma_f32_16x16x32_bf16((a),(b),(c),0,0,0)
DI s16x4 lds_tr(lds_cptr p){ return __builtin_bit_cast(s16x4, __builtin_amdgcn_ds_read_tr16_b64_v4i16((__attribute__((address_space(3))) v4i16_t*)p)); }
DI bf16x8 cat8(s16x4 a, s16x4 b){ return __builtin_bit_cast(bf16x8, __builtin_shufflevector(a,b,0,1,2,3,4,5,6,7)); }
DI float dpp_add(float v, const int ctrl_sel){
  int t;
  if(ctrl_sel==0) t=__builtin_amdgcn_mov_dpp(__float_as_int(v),0xB1,0xF,0xF,true);
  else if(ctrl_sel==1) t=__builtin_amdgcn_mov_dpp(__float_as_int(v),0x4E,0xF,0xF,true);
  else t=__builtin_amdgcn_mov_dpp(__float_as_int(v),0x141,0xF,0xF,true);
  return v+__int_as_float(t);
}
DI float lo16f(unsigned u){ return __uint_as_float(u<<16); }
DI float hi16f(unsigned u){ return __uint_as_float(u&0xffff0000u); }
DI float dot2bf(unsigned a, unsigned b, float c){
  return __builtin_amdgcn_fdot2_f32_bf16(__builtin_bit_cast(bf16x2,a),__builtin_bit_cast(bf16x2,b),c,false); }

constexpr int K1_WST=24576, K1_NST=5, K1_X=K1_WST*K1_NST, K1_LDS=K1_X+3*8192;
#define K1ST(i)

__global__ __launch_bounds__(512,2) void k1_qkv(const u16* __restrict__ x, const u16* __restrict__ wqkv,
     u16* __restrict__ Qs, u16* __restrict__ Ks, u16* __restrict__ Vt, u16* __restrict__ xn){
  extern __shared__ __attribute__((aligned(16))) char lds[];
  const int tid=threadIdx.x, lane=tid&63; const int wid=__builtin_amdgcn_readfirstlane(tid>>6);
  const int b=blockIdx.x; const int rb=(b&7)*4+((b>>3)&3), hg=b>>5;
  const unsigned lds0=(unsigned)(uintptr_t)lds;
  K1ST(0);
  const int rr=lane>>3, p=lane&7;
  const int lrow=wid*8+rr;
  const u16* xp=x+(long)(rb*64+lrow)*D+p*8;
  u32x4 xv[8];
  #pragma unroll
  for(int i=0;i<8;i++) asm volatile("global_load_dwordx4 %0, %1, off offset:%2" : "=v"(xv[i]) : "v"(xp), "n"(i*128) : "memory");
  const u16* wp=wqkv+(long)lrow*(3*D)+hg*64+(((p>>1)^(((lrow>>1)&1)|(((lrow>>3)&1)<<1)))*16)+(p&1)*8;
  asm volatile("s_barrier":::"memory");
  #pragma unroll
  for(int st=0;st<K1_NST;st++){
    #pragma unroll
    for(int ty=0;ty<3;ty++) glds16(wp+(long)st*64*(3*D)+ty*D, lds0+st*K1_WST+ty*8192+wid*1024);
  }
  asm volatile("s_waitcnt vmcnt(15)" : "+v"(xv[0]),"+v"(xv[1]),"+v"(xv[2]),"+v"(xv[3]),"+v"(xv[4]),"+v"(xv[5]),"+v"(xv[6]),"+v"(xv[7]) :: "memory");
  K1ST(1);
  float s1=0.f, s2=0.f;
  #pragma unroll
  for(int i=0;i<8;i++){
    #pragma unroll
    for(int j=0;j<4;j++){ s1=dot2bf(xv[i][j],0x3F803F80u,s1); s2=dot2bf(xv[i][j],xv[i][j],s2); }
  }
  s1=dpp_add(s1,0); s2=dpp_add(s2,0); s1=dpp_add(s1,1); s2=dpp_add(s2,1); s1=dpp_add(s1,2); s2=dpp_add(s2,2);
  const float mean=s1*(1.0f/D);
  float var=s2*(1.0f/D)-mean*mean;
  if(__builtin_expect(__any(!(mean*mean<=16.f*var)),0)){
    float q2=0.f;
    #pragma unroll
    for(int i=0;i<8;i++){
      #pragma unroll
      for(int j=0;j<4;j++){ float a=lo16f(xv[i][j])-mean, c=hi16f(xv[i][j])-mean; q2=fmaf(a,a,q2); q2=fmaf(c,c,q2); }
    }
    q2=dpp_add(q2,0); q2=dpp_add(q2,1); q2=dpp_add(q2,2);
    var=q2*(1.0f/D);
  }
  const float mb=rbf(mean);
  const float den=rbf(sqrtf(rbf(rbf(var)+1e-5f)));
  const float rinv=1.0f/den;
  char* xw=lds+K1_X+lrow*128+((p^((lrow>>1)&7))*16);
#define NORM(i) do{ u32x4 o_; _Pragma("unroll") for(int j=0;j<4;j++){ unsigned c_=pk2(lo16f(xv[i][j])-mb, hi16f(xv[i][j])-mb); o_[j]=pk2(lo16f(c_)*rinv, hi16f(c_)*rinv); } \
     xv[i]=o_; *(u32x4*)(xw+((i)%3)*8192)=o_; }while(0)
  NORM(0); NORM(1);
  K1ST(2);
  const int rt2=wid&1, hd=wid>>1;
  const int g=lane>>4, r16=lane&15;
  const int xsw=(r16>>1)&7;
  lds_cptr xa0=(lds_cptr)lds+K1_X+(rt2*32+r16)*128+((g^xsw)*16);
  lds_cptr xa1=(lds_cptr)lds+K1_X+(rt2*32+r16)*128+(((4+g)^xsw)*16);
  const int q4=(lane&15)>>2, p4=lane&3;
  const int fk=((q4>>1)&1)|((g&1)<<1);
  lds_cptr wa=(lds_cptr)lds+(8*g+q4)*128+((hd^fk)*32)+p4*8;
  f32x4 aq[2]={}, ak[2]={}, av[2]={};
  bf16x8 fx[2][2][2], fw[2][2][3];
#define RDF(c) do{ const int sl_=((c)%K1_NST)*K1_WST; const int xb_=((c)%3)*8192; \
    fx[(c)&1][0][0]=lds_ld(xa0+xb_); fx[(c)&1][0][1]=lds_ld(xa0+xb_+2048); \
    _Pragma("unroll") for(int ty=0;ty<3;ty++) fw[(c)&1][0][ty]=cat8(lds_tr(wa+sl_+ty*8192), lds_tr(wa+sl_+ty*8192+512)); \
    fx[(c)&1][1][0]=lds_ld(xa1+xb_); fx[(c)&1][1][1]=lds_ld(xa1+xb_+2048); \
    _Pragma("unroll") for(int ty=0;ty<3;ty++) fw[(c)&1][1][ty]=cat8(lds_tr(wa+sl_+ty*8192+4096), lds_tr(wa+sl_+ty*8192+4096+512)); }while(0)
  asm volatile("s_waitcnt vmcnt(12) lgkmcnt(0)\n\ts_barrier":::"memory");
  RDF(0);
#define TRW(set,s,ty,c) fw[set][s][ty]=cat8(lds_tr(wa+((c)%K1_NST)*K1_WST+(ty)*8192+(s)*4096), lds_tr(wa+((c)%K1_NST)*K1_WST+(ty)*8192+(s)*4096+512))
#define NRM1(i,j) do{ unsigned c_=pk2(lo16f(xv[i][j])-mb, hi16f(xv[i][j])-mb); xv[i][j]=pk2(lo16f(c_)*rinv, hi16f(c_)*rinv); }while(0)
  #pragma unroll
  for(int c=0;c<8;c++){
    constexpr int NW[8]={9,9,9,9,6,3,0,0};
    const int cs=c&1, ns=cs^1; const int xb=((c+1)%3)*8192; const int ni=(c+2)&7;
    if(c<7){
      asm volatile("s_waitcnt vmcnt(%0) lgkmcnt(0)\n\ts_barrier"::"n"(NW[c]):"memory");
      K1ST(3+c);
    }
    if(c<7){ fx[ns][0][0]=lds_ld(xa0+xb); fx[ns][0][1]=lds_ld(xa0+xb+2048); TRW(ns,0,0,c+1); }
    aq[0]=MFMA16(fw[cs][0][0],fx[cs][0][0],aq[0]); aq[1]=MFMA16(fw[cs][0][0],fx[cs][0][1],aq[1]); ak[0]=MFMA16(fw[cs][0][1],fx[cs][0][0],ak[0]);
    if(c<6) NRM1(ni,0);
    if(c<3) glds16(wp+(long)(c+5)*64*(3*D)+0*D, lds0+c*K1_WST+0*8192+wid*1024);
    SB();
    if(c<7){ TRW(ns,0,1,c+1); TRW(ns,0,2,c+1); }
    ak[1]=MFMA16(fw[cs][0][1],fx[cs][0][1],ak[1]); av[0]=MFMA16(fx[cs][0][0],fw[cs][0][2],av[0]); av[1]=MFMA16(fx[cs][0][1],fw[cs][0][2],av[1]);
    if(c<6) NRM1(ni,1);
    if(c<3) glds16(wp+(long)(c+5)*64*(3*D)+1*D, lds0+c*K1_WST+1*8192+wid*1024);
    SB();
    if(c<7){ fx[ns][1][0]=lds_ld(xa1+xb); fx[ns][1][1]=lds_ld(xa1+xb+2048); TRW(ns,1,0,c+1); }
    aq[0]=MFMA16(fw[cs][1][0],fx[cs][1][0],aq[0]); aq[1]=MFMA16(fw[cs][1][0],fx[cs][1][1],aq[1]); ak[0]=MFMA16(fw[cs][1][1],fx[cs][1][0],ak[0]);
    if(c<6) NRM1(ni,2);
    if(c<3) glds16(wp+(long)(c+5)*64*(3*D)+2*D, lds0+c*K1_WST+2*8192+wid*1024);
    SB();
    if(c<7){ TRW(ns,1,1,c+1); TRW(ns,1,2,c+1); }
    ak[1]=MFMA16(fw[cs][1][1],fx[cs][1][1],ak[1]); av[0]=MFMA16(fx[cs][1][0],fw[cs][1][2],av[0]); av[1]=MFMA16(fx[cs][1][1],fw[cs][1][2],av[1]);
    if(c<6){ NRM1(ni,3); *(u32x4*)(xw+(ni%3)*8192)=xv[ni]; }
    SB();
  }
  K1ST(10);
  const int h=hg*4+hd; const int T=rb*2+rt2;
  #pragma unroll
  for(int tt=0;tt<2;tt++){
    const long qo=(long)h*S*DH+T*512+(g>>1)*256+(tt*16+r16)*8+(g&1)*4;
    u32x2 wq={pk2(aq[tt][0]*QSCALE,aq[tt][1]*QSCALE),pk2(aq[tt][2]*QSCALE,aq[tt][3]*QSCALE)};
    u32x2 wk={pk2(ak[tt][0],ak[tt][1]),pk2(ak[tt][2],ak[tt][3])};
    *(u32x2*)(Qs+qo)=wq; *(u32x2*)(Ks+qo)=wk;
    const long vo=(long)h*S*DH+(2*T+tt)*256+(g&1)*128+r16*8+(g>>1)*4;
    u32x2 wv={pk2(av[tt][0],av[tt][1]),pk2(av[tt][2],av[tt][3])};
    *(u32x2*)(Vt+vo)=wv;
  }
  {
    u32x4 v=xv[0];
    #pragma unroll
    for(int i=1;i<8;i++) if(hg==i) v=xv[i];
    *(u32x4*)(xn+(long)(rb*64+lrow)*D+hg*64+p*8)=v;
  }
}

constexpr int HP=1408;
constexpr int GU_ST=32768, GU_LDS=3*GU_ST;
DI void gu_unit(char* lds, const u16* __restrict__ xn, const u16* __restrict__ wg, const u16* __restrict__ wu, u16* __restrict__ hb, int mt, int ft){
  const int tid=threadIdx.x, lane=tid&63; const int wid=__builtin_amdgcn_readfirstlane(tid>>6);
  const unsigned lds0=(unsigned)(uintptr_t)lds;
  const int rr=lane>>3, sl=lane&7;
  const int xrow=wid*8+rr;
  const u16* xs=xn+(long)(mt*128+xrow)*D+((sl^((xrow>>1)&7))*8);
  const int wk=wid*8+rr;
  int wc=ft*64+(((sl>>2)^((wk>>1)&1))*32)+(sl&3)*8; wc = wc>F-8 ? F-8 : wc;
  const u16* gs=wg+(long)wk*F+wc; const u16* us=wu+(long)wk*F+wc;
#define GU_ISSUE(c) do{ const unsigned b_=lds0+((c)%3)*GU_ST+wid*1024; \
    glds16(xs+(c)*64, b_); glds16(xs+(long)64*D+(c)*64, b_+8192); glds16(gs+(long)(c)*64*F, b_+16384); glds16(us+(long)(c)*64*F, b_+24576); }while(0)
  GU_ISSUE(0); GU_ISSUE(1);
  const int wm=wid&3, wn=wid>>2; const int r=lane&31, hh=lane>>5;
  const int xr=wm*32+r;
  lds_cptr xa=(lds_cptr)lds+xr*128; const int xsw=(xr>>1)&7;
  const int blk=(lane>>4)&1, q4=(lane&15)>>2, p4=lane&3;
  lds_cptr wa=(lds_cptr)lds+16384+(8*hh+q4)*128+((wn^((q4>>1)&1))*64)+blk*32+p4*8;
  f32x16 ag={}, au={};
  #pragma unroll
  for(int c=0;c<8;c++){
    if(c<7) asm volatile("s_waitcnt vmcnt(4) lgkmcnt(0)\n\ts_barrier":::"memory");
    else    asm volatile("s_waitcnt vmcnt(0) lgkmcnt(0)\n\ts_barrier":::"memory");
    if(c<6) GU_ISSUE(c+2);
    const int sb=(c%3)*GU_ST;
    #pragma unroll
    for(int s=0;s<4;s++){
      bf16x8 xf=lds_ld(xa+sb+(((2*s+hh)^xsw)*16));
      bf16x8 gf=cat8(lds_tr(wa+sb+s*2048), lds_tr(wa+sb+s*2048+512));
      bf16x8 uf=cat8(lds_tr(wa+sb+8192+s*2048), lds_tr(wa+sb+8192+s*2048+512));
      ag=MFMA32(gf,xf,ag); au=MFMA32(uf,xf,au);
    }
  }
  const bool valid=(ft*64+wn*32)<F;
  u32x4 o0,o1;
  #pragma unroll
  for(int i=0;i<8;i++){
    float hv[2];
    #pragma unroll
    for(int e=0;e<2;e++){ float g=ag[2*i+e]; float sg=g*__builtin_amdgcn_rcpf(1.0f+__builtin_amdgcn_exp2f(-1.4426950408889634f*g)); hv[e]=valid? sg*au[2*i+e] : 0.f; }
    unsigned w=pk2(hv[0],hv[1]); if(i<4) o0[i]=w; else o1[i-4]=w;
  }
  u16* hp=hb+(long)(mt*128+xr)*HP+ft*64+wn*32+hh*8;
  *(u32x4*)hp=o0; *(u32x4*)(hp+16)=o1;
}
__global__ __launch_bounds__(512,2) void k_gu(const u16* __restrict__ xn, const u16* __restrict__ wg, const u16* __restrict__ wu, u16* __restrict__ hb){
  extern __shared__ __attribute__((aligned(16))) char lds[];
  const int b=blockIdx.x;
  const int x=b&7, j=b>>3;
  gu_unit(lds,xn,wg,wu,hb,x*2+(j&1),j>>1);
}

constexpr int K3_ST=16384, K3_NST=8, K3_LDS=K3_ST*K3_NST;
constexpr int K3_NC=8+22;
__global__ __launch_bounds__(512,2) void k3_out(const u16* __restrict__ x, const u16* __restrict__ ob, const u16* __restrict__ hb,
      const u16* __restrict__ wo, const u16* __restrict__ wd, u16* __restrict__ out){
  extern __shared__ __attribute__((aligned(16))) char lds[];
  const int tid=threadIdx.x, lane=tid&63; const int wid=__builtin_amdgcn_readfirstlane(tid>>6);
  const unsigned lds0=(unsigned)(uintptr_t)lds;
  const int b=blockIdx.x; const int mt=(b&7)*4+((b>>3)&3), nt=b>>5;
  const int rr=lane>>3, sl=lane&7;
  const int prow=wid*8+rr;
  const int xc=(sl^((prow>>1)&7))*8;
  const u16* os=ob+(long)(mt*64+prow)*D+xc;
  const u16* hs=hb+(long)(mt*64+prow)*HP+xc;
  const int wc=nt*64+(((sl>>2)^((prow>>1)&1))*32)+(sl&3)*8;
  auto issue=[&](int c){
    const unsigned b_=lds0+(c%K3_NST)*K3_ST+wid*1024;
    if(c<8){ glds16(os+c*64,b_); glds16(wo+(long)(c*64+prow)*D+wc,b_+8192); }
    else { int k=(c-8)*64+prow; k=k>F-1?F-1:k; glds16(hs+(c-8)*64,b_); glds16(wd+(long)k*D+wc,b_+8192); }
  };
  #pragma unroll
  for(int c=0;c<7;c++) issue(c);
  const int wm=wid&1, wn=(wid>>1)&1, kh=wid>>2; const int r=lane&31, hh=lane>>5;
  const int xr=wm*32+r; const int xsw=(xr>>1)&7;
  lds_cptr xa0=(lds_cptr)lds+xr*128+(((4*kh+hh)^xsw)*16);
  lds_cptr xa1=(lds_cptr)lds+xr*128+(((4*kh+2+hh)^xsw)*16);
  const int blk=(lane>>4)&1, q4=(lane&15)>>2, p4=lane&3;
  lds_cptr wa=(lds_cptr)lds+8192+(32*kh+4*hh+q4)*128+((wn^((q4>>1)&1))*64)+blk*32+p4*8;
  f32x16 acc={};
  #pragma unroll 1
  for(int c=0;c<K3_NC;c++){
    if(c<K3_NC-6) asm volatile("s_waitcnt vmcnt(12) lgkmcnt(0)\n\ts_barrier":::"memory");
    else asm volatile("s_waitcnt vmcnt(0) lgkmcnt(0)\n\ts_barrier":::"memory");
    if(c+7<K3_NC) issue(c+7);
    const int sb=(c%K3_NST)*K3_ST;
    bf16x8 x0=lds_ld(xa0+sb), x1=lds_ld(xa1+sb);
    bf16x8 w0=cat8(lds_tr(wa+sb), lds_tr(wa+sb+1024));
    bf16x8 w1=cat8(lds_tr(wa+sb+2048), lds_tr(wa+sb+2048+1024));
    acc=MFMA32(w0,x0,acc); acc=MFMA32(w1,x1,acc);
  }
  asm volatile("s_waitcnt lgkmcnt(0)\n\ts_barrier":::"memory");
  float* xch=(float*)lds+(wid&3)*1024+lane;
  if(kh==1){
    #pragma unroll
    for(int i=0;i<16;i++) xch[i*64]=acc[i];
  }
  __syncthreads();
  if(kh==1) return;
  const long ro=(long)(mt*64+xr)*D+nt*64+wn*32+4*hh;
  #pragma unroll
  for(int q=0;q<4;q++){
    u32x2 xv=*(const u32x2*)(x+ro+8*q);
    float v0=acc[4*q]+xch[(4*q)*64]+lo16f(xv[0]), v1=acc[4*q+1]+xch[(4*q+1)*64]+hi16f(xv[0]);
    float v2=acc[4*q+2]+xch[(4*q+2)*64]+lo16f(xv[1]), v3=acc[4*q+3]+xch[(4*q+3)*64]+hi16f(xv[1]);
    u32x2 w={pk2(v0,v1),pk2(v2,v3)};
    *(u32x2*)(out+ro+8*q)=w;
  }
}

constexpr int AT_THREADS=1024; constexpr int AT_LDS_K=0, AT_LDS_V=65536, AT_LDS_ONE=131072, AT_LDS_BYTES=131072+4096;

DI void pv_step(const f32x16& sc, bf16x8 v0, bf16x8 v1, f32x16& o){
  u32x4 p0, p1;
  #pragma unroll
  for(int i=0;i<4;i++){ p0[i]=pk2(__builtin_amdgcn_exp2f(sc[2*i]),__builtin_amdgcn_exp2f(sc[2*i+1])); }
  #pragma unroll
  for(int i=0;i<4;i++){ p1[i]=pk2(__builtin_amdgcn_exp2f(sc[8+2*i]),__builtin_amdgcn_exp2f(sc[8+2*i+1])); }
  o=MFMA32(v0,__builtin_bit_cast(bf16x8,p0),o);
  o=MFMA32(v1,__builtin_bit_cast(bf16x8,p1),o);
}

#define EX(x) __builtin_amdgcn_exp2f(x)
#define STAMP(i)
__global__ __launch_bounds__(1024,4) void k_attn(const u16* __restrict__ Qs, const u16* __restrict__ Ks, const u16* __restrict__ Vt, u16* __restrict__ O){
  extern __shared__ __attribute__((aligned(16))) char lds[];
  const int tid=threadIdx.x, lane=tid&63; const int wid=__builtin_amdgcn_readfirstlane(tid>>6);
  const int b=blockIdx.x; const int h=(b&7)*4+((b>>3)>>3), qb=(b>>3)&7;
  const int r=lane&31, hh=lane>>5;
  const int qt=wid&7, kh=wid>>3;
  const unsigned lds0=(unsigned)(uintptr_t)lds;
  STAMP(0);
  const u16* qp=Qs+(long)h*S*DH+(qb*8+qt)*512+lane*8;
  bf16x8 qf;
  asm volatile("global_load_dwordx4 %0, %1, off" : "=v"(qf) : "v"(qp) : "memory");
  { const u16* g0=((qt>>2)?Vt:Ks)+(long)h*S*DH+(kh*32+(qt&3))*512+lane*8;
    const unsigned l0=lds0+((qt>>2)?AT_LDS_V:AT_LDS_K)+(kh*32+(qt&3))*1024;
    #pragma unroll
    for(int c=0;c<8;c++) glds16(g0+c*2048, l0+c*4096);
  }
  { unsigned v=((tid&63)<4)?0x3F803F80u:0u; *(unsigned*)(lds+AT_LDS_ONE+tid*4)=v; }
  lds_cptr kp0=(lds_cptr)lds+AT_LDS_K+lane*16;
  lds_cptr kp=kp0+kh*32768;
  const bool cl=(r>=16);
  lds_cptr vp= (lds_cptr)lds+(cl ? AT_LDS_ONE : AT_LDS_V+kh*32768)+hh*256+(r&15)*16;
  const int vinc= cl?0:2048;
  asm volatile("s_waitcnt vmcnt(" FIRSTW ") lgkmcnt(0)\n\ts_barrier": "+v"(qf) ::"memory");
  STAMP(1);
  f32x16 negm; f32x16 sA, sB;
  bf16x8 kA, kB, vA0, vA1, vB0, vB1;
  {
    f32x16 z={}; bf16x8 k0=lds_ld(kp0); bf16x8 k1=lds_ld(kp);
    kA=lds_ld(kp+1024); vA0=lds_ld(vp); vA1=lds_ld(vp+512);
    f32x16 s0=MFMA32(k0,qf,z);
    float m=s0[0];
    #pragma unroll
    for(int i=1;i<16;i++) m=fmaxf(m,s0[i]);
    auto rr=__builtin_amdgcn_permlane32_swap(__float_as_uint(m),__float_as_uint(m),false,false);
    m=fmaxf(__uint_as_float(rr[0]),__uint_as_float(rr[1]));
    float nm=-(m+60.f);
    #pragma unroll
    for(int i=0;i<16;i++){ negm[i]=nm; }
    sA=MFMA32(k1,qf,negm);
  }
  f32x16 o={};
#define STEP(sC,sN,kU,kL,vU0,vU1,vL0,vL1,KOFF,VOFF) do{ \
    u32x4 p0,p1; \
    kL=lds_ld(kp+(KOFF)); vL0=lds_ld(vp+(VOFF)); vL1=lds_ld(vp+(VOFF)+512); \
    float e0=EX(sC[0]),e1=EX(sC[1]),e2=EX(sC[2]),e3=EX(sC[3]); SB(); \
    sN=MFMA32(kU,qf,negm); SB(); \
    float e4=EX(sC[4]),e5=EX(sC[5]),e6=EX(sC[6]),e7=EX(sC[7]); \
    p0[0]=pk2(e0,e1); p0[1]=pk2(e2,e3); p0[2]=pk2(e4,e5); p0[3]=pk2(e6,e7); SB(); \
    float e8=EX(sC[8]),e9=EX(sC[9]); SB(); \
    o=MFMA32(vU0,__builtin_bit_cast(bf16x8,p0),o); SB(); \
    float e10=EX(sC[10]),e11=EX(sC[11]),e12=EX(sC[12]),e13=EX(sC[13]),e14=EX(sC[14]),e15=EX(sC[15]); \
    p1[0]=pk2(e8,e9); p1[1]=pk2(e10,e11); p1[2]=pk2(e12,e13); p1[3]=pk2(e14,e15); SB(); \
    o=MFMA32(vU1,__builtin_bit_cast(bf16x8,p1),o); SB(); \
  }while(0)
#define ITER() do{ STEP(sA,sB,kA,kB,vA0,vA1,vB0,vB1,2048,1024); STEP(sB,sA,kB,kA,vB0,vB1,vA0,vA1,3072,2048); kp+=2048; vp+=vinc; }while(0)
  #pragma unroll 1
  for(int it=0;it<16;it++){ ITER(); }
  STAMP(2);
  __syncthreads();
  float* xch=(float*)lds + qt*64*9 + lane;
  if(kh==1){
    #pragma unroll
    for(int i=0;i<9;i++) xch[i*64]=o[i];
  }
  __syncthreads();
  if(kh==1) return;
  #pragma unroll
  for(int i=0;i<9;i++) o[i]+=xch[i*64];
  float l=o[8];
  { auto rr=__builtin_amdgcn_permlane32_swap(__float_as_uint(l),__float_as_uint(l),false,false); l=__uint_as_float(rr[0]); }
  bool bad = !(l<1e30f);
  #pragma unroll
  for(int i=0;i<8;i++) bad = bad || !(fabsf(o[i])<1e37f);
  if(__builtin_expect(__any(bad),0)){
    const u16* kgl=Ks+(long)h*S*DH+lane*8;
    const u16* vgl=Vt+(long)h*S*DH+hh*128+(r&15)*8;
    float m=-INFINITY; f32x16 z={};
    #pragma unroll
    for(int i=0;i<16;i++) o[i]=0.f;
    for(int t=0;t<64;t++){
      bf16x8 kf=*(const bf16x8*)(kgl+t*512);
      bf16x8 v0=*(const bf16x8*)(vgl+t*512), v1=*(const bf16x8*)(vgl+t*512+256);
      if(cl){ u32x4 one={0x3F803F80u,0x3F803F80u,0x3F803F80u,0x3F803F80u}; v0=__builtin_bit_cast(bf16x8,one); v1=v0; }
      f32x16 s0=MFMA32(kf,qf,z);
      float tm=s0[0];
      #pragma unroll
      for(int i=1;i<16;i++) tm=fmaxf(tm,s0[i]);
      auto rr=__builtin_amdgcn_permlane32_swap(__float_as_uint(tm),__float_as_uint(tm),false,false);
      tm=fmaxf(__uint_as_float(rr[0]),__uint_as_float(rr[1]));
      float mn=fmaxf(m,tm);
      float alpha=__builtin_amdgcn_exp2f(m-mn); m=mn;
      #pragma unroll
      for(int i=0;i<16;i++){ o[i]*=alpha; s0[i]-=mn; }
      pv_step(s0,v0,v1,o);
    }
    l=o[8];
    auto rr=__builtin_amdgcn_permlane32_swap(__float_as_uint(l),__float_as_uint(l),false,false); l=__uint_as_float(rr[0]);
  }
  float rl=1.0f/l;
  u32x2 w0={pk2(o[0]*rl,o[1]*rl),pk2(o[2]*rl,o[3]*rl)};
  u32x2 w1={pk2(o[4]*rl,o[5]*rl),pk2(o[6]*rl,o[7]*rl)};
  u16* op=O+(long)((qb*8+qt)*32+r)*D+h*DH+hh*8;
  *(u32x4*)op=(u32x4){w0[0],w0[1],w1[0],w1[1]};
  STAMP(3);
}

__global__ void k_swiglu(const float* g, const float* u, u16* hh, int n){
  int i=blockIdx.x*256+threadIdx.x; if(i>=n) return;
  float gg=g[i]; float s=gg/(1.f+expf(-gg)); hh[i]=f2bf(s*u[i]);
}
__global__ void k_final(const u16* x, const float* a, const float* b, u16* out, int n){
  int i=blockIdx.x*256+threadIdx.x; if(i>=n) return;
  out[i]=f2bf(bf2f(x[i])+a[i]+b[i]);
}
extern "C" void kernel_launch(void* const* d_in, const int* in_sizes, int n_in, void* d_out, int out_size, void* d_ws, size_t ws_size, hipStream_t stream){
  const u16* x=(const u16*)d_in[0]; const u16* wqkv=(const u16*)d_in[1]; const u16* wo=(const u16*)d_in[2];
  const u16* wg=(const u16*)d_in[4]; const u16* wu=(const u16*)d_in[5]; const u16* wd=(const u16*)d_in[6];
  char* ws=(char*)d_ws;
  u16* xn=(u16*)ws; ws+=S*D*2;
  u16* hb=(u16*)ws; ws+=(size_t)S*HP*2;
  u16* o=(u16*)ws; ws+=(size_t)S*D*2;
  u16* Qs=(u16*)ws; ws+=(size_t)S*D*2;
  u16* Ks=(u16*)ws; ws+=(size_t)S*D*2;
  u16* Vt=(u16*)ws; ws+=(size_t)S*D*2;
  (void)hipFuncSetAttribute((const void*)k_attn, hipFuncAttributeMaxDynamicSharedMemorySize, AT_LDS_BYTES);
  (void)hipFuncSetAttribute((const void*)k1_qkv, hipFuncAttributeMaxDynamicSharedMemorySize, K1_LDS);
  (void)hipFuncSetAttribute((const void*)k_gu, hipFuncAttributeMaxDynamicSharedMemorySize, GU_LDS);
  (void)hipFuncSetAttribute((const void*)k3_out, hipFuncAttributeMaxDynamicSharedMemorySize, K3_LDS);
  k1_qkv<<<256,512,K1_LDS,stream>>>(x,wqkv,Qs,Ks,Vt,xn);
  k_gu<<<352,512,GU_LDS,stream>>>(xn,wg,wu,hb);
  k_attn<<<256,1024,AT_LDS_BYTES,stream>>>(Qs,Ks,Vt,o);
  k3_out<<<256,512,K3_LDS,stream>>>(x,o,hb,wo,wd,(u16*)d_out);
}
```

```cpp
#define K1MAP 0
#define GU_DELAY 3
#define ABL 0
#define NBLK (256+352)
#define PACE 0
#define BARMODE 0
#define FIRSTW "0"
#include <hip/hip_runtime.h>
#include <hip/hip_bf16.h>
#include <stdint.h>

typedef unsigned short u16;
typedef __attribute__((ext_vector_type(8))) __bf16 bf16x8;
typedef __attribute__((ext_vector_type(2))) __bf16 bf16x2;
typedef __attribute__((ext_vector_type(2))) float f32x2;
typedef __attribute__((ext_vector_type(16))) float f32x16;
typedef __attribute__((ext_vector_type(4))) unsigned u32x4;
typedef __attribute__((ext_vector_type(2))) unsigned u32x2;
#define DI __device__ __forceinline__
#define MFMA32(a,b,c) __builtin_amdgcn_mfma_f32_32x32x16_bf16((a),(b),(c),0,0,0)

DI float bf2f(u16 b){ return __uint_as_float(((unsigned)b)<<16); }
DI u16 f2bf(float f){ unsigned u=__float_as_uint(f); u += 0x7fffu + ((u>>16)&1u); return (u16)(u>>16); }
DI float rbf(float f){ return bf2f(f2bf(f)); }
DI unsigned pk2(float lo, float hi){ f32x2 v={lo,hi}; bf16x2 b=__builtin_convertvector(v,bf16x2); return __builtin_bit_cast(unsigned,b); }

constexpr int S=2048, D=512, H=32, DH=16, F=1376;
constexpr float QSCALE = 0.25f*1.4426950408889634f;

DI void glds16(const void* g, unsigned lds_base) {
  unsigned sv; asm volatile("s_mov_b32 %0, m0\n\ts_mov_b32 m0, %2\n\ts_nop 0\n\tglobal_load_lds_dwordx4 %1, off\n\ts_mov_b32 m0, %0" : "=&s"(sv) : "v"(g), "s"(lds_base) : "memory"); }

typedef __attribute__((address_space(3))) const char* lds_cptr;
DI bf16x8 lds_ld(lds_cptr p){ return *(const __attribute__((address_space(3))) bf16x8*)p; }

#define SB() __builtin_amdgcn_sched_barrier(0)
typedef __attribute__((ext_vector_type(4))) float f32x4;
typedef __attribute__((ext_vector_type(4))) short s16x4;
typedef short v4i16_t __attribute__((ext_vector_type(4)));
#define MFMA16(a,b,c) __builtin_amdgcn_mfma_f32_16x16x32_bf16((a),(b),(c),0,0,0)
DI s16x4 lds_tr(lds_cptr p){ return __builtin_bit_cast(s16x4, __builtin_amdgcn_ds_read_tr16_b64_v4i16((__attribute__((address_space(3))) v4i16_t*)p)); }
DI bf16x8 cat8(s16x4 a, s16x4 b){ return __builtin_bit_cast(bf16x8, __builtin_shufflevector(a,b,0,1,2,3,4,5,6,7)); }
DI float dpp_add(float v, const int ctrl_sel){
  int t;
  if(ctrl_sel==0) t=__builtin_amdgcn_mov_dpp(__float_as_int(v),0xB1,0xF,0xF,true);
  else if(ctrl_sel==1) t=__builtin_amdgcn_mov_dpp(__float_as_int(v),0x4E,0xF,0xF,true);
  else t=__builtin_amdgcn_mov_dpp(__float_as_int(v),0x141,0xF,0xF,true);
  return v+__int_as_float(t);
}
DI float lo16f(unsigned u){ return __uint_as_float(u<<16); }
DI float hi16f(unsigned u){ return __uint_as_float(u&0xffff0000u); }
DI float dot2bf(unsigned a, unsigned b, float c){
  return __builtin_amdgcn_fdot2_f32_bf16(__builtin_bit_cast(bf16x2,a),__builtin_bit_cast(bf16x2,b),c,false); }

constexpr int HP=1408;
constexpr int K1_WST=24576, K1_NST=4, K1_X=K1_WST*K1_NST, K1_LDS=K1_X+8*8192;

template<int NT> DI void gu_stage(lds_cptr xa, lds_cptr wa, int xo0, int xo1, f32x16 (&ag)[2], f32x16 (&au)[2]){
  #pragma unroll
  for(int s=0;s<2;s++){
    bf16x8 gf=cat8(lds_tr(wa+s*6144), lds_tr(wa+s*6144+1536));
    bf16x8 uf=cat8(lds_tr(wa+12288+s*6144), lds_tr(wa+12288+s*6144+1536));
    #pragma unroll
    for(int t=0;t<NT;t++){
      bf16x8 xf=lds_ld(xa+t*4096+(s?xo1:xo0));
      ag[t]=MFMA32(gf,xf,ag[t]); au[t]=MFMA32(uf,xf,au[t]);
    }
  }
}
DI void gu_store(const f32x16& ag, const f32x16& au, u16* hp, bool zero){
  u32x4 o0,o1;
  #pragma unroll
  for(int i=0;i<8;i++){
    float hv[2];
    #pragma unroll
    for(int e=0;e<2;e++){ float g=ag[2*i+e]; float sg=g*__builtin_amdgcn_rcpf(1.0f+__builtin_amdgcn_exp2f(-1.4426950408889634f*g)); hv[e]=zero?0.f:sg*au[2*i+e]; }
    unsigned w=pk2(hv[0],hv[1]); if(i<4) o0[i]=w; else o1[i-4]=w;
  }
  *(u32x4*)hp=o0; *(u32x4*)(hp+16)=o1;
}

__global__ __launch_bounds__(512,2) void k1_qkv(const u16* __restrict__ x, const u16* __restrict__ wqkv,
     u16* __restrict__ Qs, u16* __restrict__ Ks, u16* __restrict__ Vt,
     const u16* __restrict__ wg, const u16* __restrict__ wu, u16* __restrict__ hb){
  extern __shared__ __attribute__((aligned(16))) char lds[];
  const int tid=threadIdx.x, lane=tid&63; const int wid=__builtin_amdgcn_readfirstlane(tid>>6);
  const int b=blockIdx.x;
  const int rb=(b&7)*4+((b>>3)&3), hg=b>>5;
  const unsigned lds0=(unsigned)(uintptr_t)lds;
  const int rr=lane>>3, p=lane&7;
  const int lrow=wid*8+rr;
  const u16* xp=x+(long)(rb*64+lrow)*D+p*8;
  u32x4 xv[8];
  #pragma unroll
  for(int i=0;i<8;i++) asm volatile("global_load_dwordx4 %0, %1, off offset:%2" : "=v"(xv[i]) : "v"(xp), "n"(i*128) : "memory");
  const u16* wp=wqkv+(long)lrow*(3*D)+hg*64+(((p>>1)^(((lrow>>1)&1)|(((lrow>>3)&1)<<1)))*16)+(p&1)*8;
  asm volatile("s_barrier":::"memory");
  #pragma unroll
  for(int st=0;st<K1_NST;st++){
    #pragma unroll
    for(int ty=0;ty<3;ty++) glds16(wp+(long)st*64*(3*D)+ty*D, lds0+st*K1_WST+ty*8192+wid*1024);
  }
  const int ft0=hg*5+(hg<3?hg:3);
  int goff[3];
  #pragma unroll
  for(int i=0;i<3;i++){
    const int pc=wid*3+i; const int i16=(pc%12)*64+lane; const int k=i16/24, ch=i16%24;
    int col=ft0*32+(((ch>>2)^((k>>1)&1))*32)+(ch&3)*8; col=col>F-8?F-8:col;
    goff[i]=k*F+col;
  }
  const u16* gbase=(wid<4)?wg:wu;
#define GU_ISS(g_) do{ _Pragma("unroll") for(int i=0;i<3;i++) glds16(gbase+(long)(g_)*32*F+goff[i], lds0+((g_)&3)*K1_WST+(wid*3+i)*1024); }while(0)
  asm volatile("s_waitcnt vmcnt(12)" : "+v"(xv[0]),"+v"(xv[1]),"+v"(xv[2]),"+v"(xv[3]),"+v"(xv[4]),"+v"(xv[5]),"+v"(xv[6]),"+v"(xv[7]) :: "memory");
  float s1=0.f, s2=0.f;
  #pragma unroll
  for(int i=0;i<8;i++){
    #pragma unroll
    for(int j=0;j<4;j++){ s1=dot2bf(xv[i][j],0x3F803F80u,s1); s2=dot2bf(xv[i][j],xv[i][j],s2); }
  }
  s1=dpp_add(s1,0); s2=dpp_add(s2,0); s1=dpp_add(s1,1); s2=dpp_add(s2,1); s1=dpp_add(s1,2); s2=dpp_add(s2,2);
  const float mean=s1*(1.0f/D);
  float var=s2*(1.0f/D)-mean*mean;
  if(__builtin_expect(__any(!(mean*mean<=16.f*var)),0)){
    float q2=0.f;
    #pragma unroll
    for(int i=0;i<8;i++){
      #pragma unroll
      for(int j=0;j<4;j++){ float a=lo16f(xv[i][j])-mean, c=hi16f(xv[i][j])-mean; q2=fmaf(a,a,q2); q2=fmaf(c,c,q2); }
    }
    q2=dpp_add(q2,0); q2=dpp_add(q2,1); q2=dpp_add(q2,2);
    var=q2*(1.0f/D);
  }
  const float mb=rbf(mean);
  const float den=rbf(sqrtf(rbf(rbf(var)+1e-5f)));
  const float rinv=1.0f/den;
  char* xw=lds+K1_X+lrow*128+((p^((lrow>>1)&7))*16);
#define NRM1(i,j) do{ unsigned c_=pk2(lo16f(xv[i][j])-mb, hi16f(xv[i][j])-mb); xv[i][j]=pk2(lo16f(c_)*rinv, hi16f(c_)*rinv); }while(0)
#define NORM(i) do{ NRM1(i,0); NRM1(i,1); NRM1(i,2); NRM1(i,3); *(u32x4*)(xw+(i)*8192)=xv[i]; }while(0)
  NORM(0); NORM(1);
  const int rt2=wid&1, hd=wid>>1;
  const int g=lane>>4, r16=lane&15;
  const int xsw=(r16>>1)&7;
  lds_cptr xa0=(lds_cptr)lds+K1_X+(rt2*32+r16)*128+((g^xsw)*16);
  lds_cptr xa1=(lds_cptr)lds+K1_X+(rt2*32+r16)*128+(((4+g)^xsw)*16);
  const int q4=(lane&15)>>2, p4=lane&3;
  const int fk=((q4>>1)&1)|((g&1)<<1);
  lds_cptr wa=(lds_cptr)lds+(8*g+q4)*128+((hd^fk)*32)+p4*8;
  f32x4 aq[2]={}, ak[2]={}, av[2]={};
  bf16x8 fx[2][2][2], fw[2][2][3];
#define TRW(set,s,ty,c) fw[set][s][ty]=cat8(lds_tr(wa+((c)%K1_NST)*K1_WST+(ty)*8192+(s)*4096), lds_tr(wa+((c)%K1_NST)*K1_WST+(ty)*8192+(s)*4096+512))
  asm volatile("s_waitcnt vmcnt(9) lgkmcnt(0)\n\ts_barrier":::"memory");
  fx[0][0][0]=lds_ld(xa0); fx[0][0][1]=lds_ld(xa0+2048); fx[0][1][0]=lds_ld(xa1); fx[0][1][1]=lds_ld(xa1+2048);
  TRW(0,0,0,0); TRW(0,0,1,0); TRW(0,0,2,0); TRW(0,1,0,0); TRW(0,1,1,0); TRW(0,1,2,0);
#define K1_ISS1(c,i) do{ if((c)<4) glds16(wp+(long)((c)+4)*64*(3*D)+(i)*D, lds0+(c)*K1_WST+(i)*8192+wid*1024); \
                        else if((c)<7) glds16(gbase+(long)((c)-4)*32*F+goff[i], lds0+((c)-4)*K1_WST+(wid*3+(i))*1024); }while(0)
  #pragma unroll
  for(int c=0;c<8;c++){
    const int cs=c&1, ns=cs^1; const int xb=(c+1)*8192; const int ni=(c+2)&7;
    if(c<7) asm volatile("s_waitcnt vmcnt(6) lgkmcnt(0)\n\ts_barrier":::"memory");
    if(c<7){ fx[ns][0][0]=lds_ld(xa0+xb); fx[ns][0][1]=lds_ld(xa0+xb+2048); TRW(ns,0,0,c+1); }
    aq[0]=MFMA16(fw[cs][0][0],fx[cs][0][0],aq[0]); aq[1]=MFMA16(fw[cs][0][0],fx[cs][0][1],aq[1]); ak[0]=MFMA16(fw[cs][0][1],fx[cs][0][0],ak[0]);
    if(c<6) NRM1(ni,0);
    K1_ISS1(c,0);
    SB();
    if(c<7){ TRW(ns,0,1,c+1); TRW(ns,0,2,c+1); }
    ak[1]=MFMA16(fw[cs][0][1],fx[cs][0][1],ak[1]); av[0]=MFMA16(fx[cs][0][0],fw[cs][0][2],av[0]); av[1]=MFMA16(fx[cs][0][1],fw[cs][0][2],av[1]);
    if(c<6) NRM1(ni,1);
    K1_ISS1(c,1);
    SB();
    if(c<7){ fx[ns][1][0]=lds_ld(xa1+xb); fx[ns][1][1]=lds_ld(xa1+xb+2048); TRW(ns,1,0,c+1); }
    aq[0]=MFMA16(fw[cs][1][0],fx[cs][1][0],aq[0]); aq[1]=MFMA16(fw[cs][1][0],fx[cs][1][1],aq[1]); ak[0]=MFMA16(fw[cs][1][1],fx[cs][1][0],ak[0]);
    if(c<6) NRM1(ni,2);
    K1_ISS1(c,2);
    SB();
    if(c<7){ TRW(ns,1,1,c+1); TRW(ns,1,2,c+1); }
    ak[1]=MFMA16(fw[cs][1][1],fx[cs][1][1],ak[1]); av[0]=MFMA16(fx[cs][1][0],fw[cs][1][2],av[0]); av[1]=MFMA16(fx[cs][1][1],fw[cs][1][2],av[1]);
    if(c<6){ NRM1(ni,3); *(u32x4*)(xw+ni*8192)=xv[ni]; }
    SB();
  }
  {
    const int h=hg*4+hd; const int T=rb*2+rt2;
    #pragma unroll
    for(int tt=0;tt<2;tt++){
      const long qo=(long)h*S*DH+T*512+(g>>1)*256+(tt*16+r16)*8+(g&1)*4;
      u32x2 wq={pk2(aq[tt][0]*QSCALE,aq[tt][1]*QSCALE),pk2(aq[tt][2]*QSCALE,aq[tt][3]*QSCALE)};
      u32x2 wk={pk2(ak[tt][0],ak[tt][1]),pk2(ak[tt][2],ak[tt][3])};
      const long vo=(long)h*S*DH+(2*T+tt)*256+(g&1)*128+r16*8+(g>>1)*4;
      u32x2 wv={pk2(av[tt][0],av[tt][1]),pk2(av[tt][2],av[tt][3])};
      asm volatile("global_store_dwordx2 %0, %1, off\n\tglobal_store_dwordx2 %2, %3, off\n\tglobal_store_dwordx2 %4, %5, off"
         :: "v"(Qs+qo),"v"(wq),"v"(Ks+qo),"v"(wk),"v"(Vt+vo),"v"(wv) : "memory");
    }
  }
  {
    const int r=lane&31, hh=lane>>5;
    const int ftl = wid<4 ? wid : 4+((wid-4)>>1);
    const int tt0 = wid<4 ? 0 : ((wid-4)&1);
    const int xsw2=(r>>1)&7;
    lds_cptr xg=(lds_cptr)lds+K1_X+(tt0*32+r)*128;
    const int blk=(lane>>4)&1;
    lds_cptr wg_=(lds_cptr)lds+(8*hh+q4)*384+((ftl^((q4>>1)&1))*64)+blk*32+p4*8;
    f32x16 ag[2]={}, au[2]={};
    bf16x8 gf[2][2], uf[2][2], xf[2][2][2];
    const bool two=(wid<4);
#define GRD(gs_,s) do{ const int st_=(gs_)&1; lds_cptr w_=wg_+((gs_)&3)*K1_WST+(s)*6144; \
      gf[st_][s]=cat8(lds_tr(w_), lds_tr(w_+1536)); uf[st_][s]=cat8(lds_tr(w_+12288), lds_tr(w_+12288+1536)); \
      lds_cptr x_=xg+((gs_)>>1)*8192+(((4*((gs_)&1)+2*(s)+hh)^xsw2)*16); xf[st_][s][0]=lds_ld(x_); if(two) xf[st_][s][1]=lds_ld(x_+4096); }while(0)
#define GMM(gs_,s) do{ const int st_=(gs_)&1; ag[0]=MFMA32(gf[st_][s],xf[st_][s][0],ag[0]); au[0]=MFMA32(uf[st_][s],xf[st_][s][0],au[0]); \
      if(two){ ag[1]=MFMA32(gf[st_][s],xf[st_][s][1],ag[1]); au[1]=MFMA32(uf[st_][s],xf[st_][s][1],au[1]); } }while(0)
#define GU_ISS1(g_,i) glds16(gbase+(long)(g_)*32*F+goff[i], lds0+((g_)&3)*K1_WST+(wid*3+(i))*1024)
    asm volatile("s_waitcnt vmcnt(12) lgkmcnt(0)\n\ts_barrier":::"memory");
    GRD(0,0); GRD(0,1);
    GU_ISS(3);
    #pragma unroll
    for(int gs=0;gs<16;gs++){
      if(gs==0) asm volatile("s_waitcnt vmcnt(12) lgkmcnt(0)\n\ts_barrier":::"memory");
      else if(gs==1) asm volatile("s_waitcnt vmcnt(12) lgkmcnt(0)\n\ts_barrier":::"memory");
      else if(gs<13) asm volatile("s_waitcnt vmcnt(6) lgkmcnt(0)\n\ts_barrier":::"memory");
      else if(gs==13) asm volatile("s_waitcnt vmcnt(3) lgkmcnt(0)\n\ts_barrier":::"memory");
      else if(gs==14) asm volatile("s_waitcnt vmcnt(0) lgkmcnt(0)\n\ts_barrier":::"memory");
      if(gs<15) GRD(gs+1,0);
      GMM(gs,0);
      if(gs+4<16){ GU_ISS1(gs+4,0); GU_ISS1(gs+4,1); }
      SB();
      if(gs<15) GRD(gs+1,1);
      GMM(gs,1);
      if(gs+4<16){ GU_ISS1(gs+4,2); }
      SB();
    }
    const int ft=ft0+ftl;
    const bool own=(ftl<5)||(hg<3)||(hg==7);
    if(own){
      u16* hp=hb+(long)(rb*64+tt0*32+r)*HP+ft*32+hh*8;
      gu_store(ag[0],au[0],hp,ft>=43);
      if(wid<4) gu_store(ag[1],au[1],hp+(long)32*HP,false);
    }
  }
}

constexpr int K3_LDS=131072;
__global__ __launch_bounds__(512,2) void k3_out(const float* __restrict__ yb, const u16* __restrict__ ob, const u16* __restrict__ wo, u16* __restrict__ out){
  extern __shared__ __attribute__((aligned(16))) char lds[];
  const int tid=threadIdx.x, lane=tid&63; const int wid=__builtin_amdgcn_readfirstlane(tid>>6);
  const unsigned lds0=(unsigned)(uintptr_t)lds;
  const int b=blockIdx.x; const int mt=(b&7)*4+((b>>3)&3), nt=b>>5;
  const int rr=lane>>3, sl=lane&7;
  const int prow=wid*8+rr;
  const u16* os=ob+(long)(mt*64+prow)*D+(sl^((prow>>1)&7))*8;
  const u16* ws_=wo+(long)prow*D+nt*64+(((sl>>2)^((prow>>1)&1))*32)+(sl&3)*8;
  #pragma unroll
  for(int c=0;c<8;c++){ glds16(os+c*64, lds0+c*16384+wid*1024); glds16(ws_+(long)c*64*D, lds0+c*16384+8192+wid*1024); }
  const int wm=wid&1, wn=(wid>>1)&1, kh=wid>>2; const int r=lane&31, hh=lane>>5;
  const int xr=wm*32+r; const int xsw=(xr>>1)&7;
  lds_cptr xa0=(lds_cptr)lds+xr*128+(((4*kh+hh)^xsw)*16);
  lds_cptr xa1=(lds_cptr)lds+xr*128+(((4*kh+2+hh)^xsw)*16);
  const int blk=(lane>>4)&1, q4=(lane&15)>>2, p4=lane&3;
  lds_cptr wa=(lds_cptr)lds+8192+(32*kh+4*hh+q4)*128+((wn^((q4>>1)&1))*64)+blk*32+p4*8;
  const long ro=(long)(mt*64+xr)*D+nt*64+wn*32+4*hh;
  f32x4 yv[4];
  if(kh==0){
    #pragma unroll
    for(int q=0;q<4;q++) asm volatile("global_load_dwordx4 %0, %1, off offset:%2" : "=v"(yv[q]) : "v"(yb+ro), "n"(q*32) : "memory");
  }
  f32x16 acc={};
  #pragma unroll
  for(int half=0;half<2;half++){
    if(half==0){ if(kh==0) asm volatile("s_waitcnt vmcnt(12)":::"memory"); else asm volatile("s_waitcnt vmcnt(8)":::"memory"); asm volatile("s_barrier":::"memory"); }
    else asm volatile("s_waitcnt vmcnt(0)\n\ts_barrier":"+v"(yv[0]),"+v"(yv[1]),"+v"(yv[2]),"+v"(yv[3])::"memory");
    #pragma unroll
    for(int c=half*4;c<half*4+4;c++){
      const int sb=c*16384;
      bf16x8 x0=lds_ld(xa0+sb), x1=lds_ld(xa1+sb);
      bf16x8 w0=cat8(lds_tr(wa+sb), lds_tr(wa+sb+1024));
      bf16x8 w1=cat8(lds_tr(wa+sb+2048), lds_tr(wa+sb+2048+1024));
      acc=MFMA32(w0,x0,acc); acc=MFMA32(w1,x1,acc);
    }
  }
  asm volatile("s_waitcnt lgkmcnt(0)\n\ts_barrier":::"memory");
  float* xch=(float*)lds+(wid&3)*1024+lane;
  if(kh==1){
    #pragma unroll
    for(int i=0;i<16;i++) xch[i*64]=acc[i];
  }
  __syncthreads();
  if(kh==1) return;
  #pragma unroll
  for(int q=0;q<4;q++){
    float v0=acc[4*q]+xch[(4*q)*64]+yv[q][0], v1=acc[4*q+1]+xch[(4*q+1)*64]+yv[q][1];
    float v2=acc[4*q+2]+xch[(4*q+2)*64]+yv[q][2], v3=acc[4*q+3]+xch[(4*q+3)*64]+yv[q][3];
    u32x2 w={pk2(v0,v1),pk2(v2,v3)};
    *(u32x2*)(out+ro+8*q)=w;
  }
}
#define GU_PRIO 2
constexpr int A2_K=0, A2_V=32768, A2_C=65536, K2_LDS=65536+4096;
#define EX(x) __builtin_amdgcn_exp2f(x)
DI void pv_step(const f32x16& sc, bf16x8 v0, bf16x8 v1, f32x16& o){
  u32x4 p0, p1;
  #pragma unroll
  for(int i=0;i<4;i++){ p0[i]=pk2(EX(sc[2*i]),EX(sc[2*i+1])); }
  #pragma unroll
  for(int i=0;i<4;i++){ p1[i]=pk2(EX(sc[8+2*i]),EX(sc[8+2*i+1])); }
  o=MFMA32(v0,__builtin_bit_cast(bf16x8,p0),o);
  o=MFMA32(v1,__builtin_bit_cast(bf16x8,p1),o);
}
DI void attn_role(char* lds, const u16* __restrict__ Qs, const u16* __restrict__ Ks, const u16* __restrict__ Vt, u16* __restrict__ O, int b){
  const int tid=threadIdx.x, lane=tid&63; const int wid=__builtin_amdgcn_readfirstlane(tid>>6);
  const int h=(b&7)*4+((b>>3)>>3), qb=(b>>3)&7;
  const int r=lane&31, hh=lane>>5;
  const unsigned lds0=(unsigned)(uintptr_t)lds;
  const u16* qp=Qs+(long)h*S*DH+(qb*8+wid)*512+lane*8;
  bf16x8 qf;
  asm volatile("global_load_dwordx4 %0, %1, off" : "=v"(qf) : "v"(qp) : "memory");
  const u16* kg=Ks+(long)h*S*DH+wid*512+lane*8;
  const u16* vg=Vt+(long)h*S*DH+wid*512+lane*8;
#define A_ISSUE(c) do{ glds16(kg+(c)*4096, lds0+A2_K+((c)&3)*8192+wid*1024); glds16(vg+(c)*4096, lds0+A2_V+((c)&3)*8192+wid*1024); }while(0)
  A_ISSUE(0); A_ISSUE(1); A_ISSUE(2); A_ISSUE(3);
  { u32x2 v; v[0]=((tid&31)<2)?0x3F803F80u:0u; v[1]=v[0]; *(u32x2*)(lds+A2_C+tid*8)=v; }
  lds_cptr kp=(lds_cptr)lds+A2_K+lane*16;
  const bool cl=(r>=16);
  lds_cptr vp=(lds_cptr)lds+(cl ? A2_C : A2_V)+hh*256+(r&15)*16;
  const int vinc= cl?0:1024; const int vwrap= cl?0:(1024-32768);
  asm volatile("s_waitcnt vmcnt(6) lgkmcnt(0)\n\ts_barrier": "+v"(qf) ::"memory");
  f32x16 negm; f32x16 sA, sB;
  bf16x8 kA, kB, vA0, vA1, vB0, vB1;
  {
    f32x16 z={}; bf16x8 k0=lds_ld(kp);
    kA=lds_ld(kp+1024); vA0=lds_ld(vp); vA1=lds_ld(vp+512); vp+=vinc;
    f32x16 s0=MFMA32(k0,qf,z);
    float m=s0[0];
    #pragma unroll
    for(int i=1;i<16;i++) m=fmaxf(m,s0[i]);
    auto rr=__builtin_amdgcn_permlane32_swap(__float_as_uint(m),__float_as_uint(m),false,false);
    m=fmaxf(__uint_as_float(rr[0]),__uint_as_float(rr[1]));
    float nm=-(m+60.f);
    #pragma unroll
    for(int i=0;i<16;i++){ negm[i]=nm; sA[i]=s0[i]+nm; }
  }
  f32x16 o={};
#define STEP(sC,sN,kU,kL,vU0,vU1,vL0,vL1,t) do{ \
    u32x4 p0,p1; \
    kL=lds_ld(kp+(((t)+2)&31)*1024); vL0=lds_ld(vp); vL1=lds_ld(vp+512); vp+=((((t)+1)&31)==31)?vwrap:vinc; \
    float e0=EX(sC[0]),e1=EX(sC[1]),e2=EX(sC[2]),e3=EX(sC[3]); SB(); \
    sN=MFMA32(kU,qf,negm); SB(); \
    float e4=EX(sC[4]),e5=EX(sC[5]),e6=EX(sC[6]),e7=EX(sC[7]); \
    p0[0]=pk2(e0,e1); p0[1]=pk2(e2,e3); p0[2]=pk2(e4,e5); p0[3]=pk2(e6,e7); SB(); \
    float e8=EX(sC[8]),e9=EX(sC[9]); SB(); \
    o=MFMA32(vU0,__builtin_bit_cast(bf16x8,p0),o); SB(); \
    float e10=EX(sC[10]),e11=EX(sC[11]),e12=EX(sC[12]),e13=EX(sC[13]),e14=EX(sC[14]),e15=EX(sC[15]); \
    p1[0]=pk2(e8,e9); p1[1]=pk2(e10,e11); p1[2]=pk2(e12,e13); p1[3]=pk2(e14,e15); SB(); \
    o=MFMA32(vU1,__builtin_bit_cast(bf16x8,p1),o); SB(); \
  }while(0)
#define ITER(t) do{ STEP(sA,sB,kA,kB,vA0,vA1,vB0,vB1,(t)); STEP(sB,sA,kB,kA,vB0,vB1,vA0,vA1,(t)+1); }while(0)
  #pragma unroll
  for(int j=0;j<8;j++){
    ITER(8*j); ITER(8*j+2); ITER(8*j+4);
    if(j<7){
      if(j==0) asm volatile("s_waitcnt vmcnt(4) lgkmcnt(0)\n\ts_barrier":::"memory");
      else if(j<6) asm volatile("s_waitcnt vmcnt(2) lgkmcnt(0)\n\ts_barrier":::"memory");
      else asm volatile("s_waitcnt vmcnt(0) lgkmcnt(0)\n\ts_barrier":::"memory");
      if(j>=1 && j+3<8) A_ISSUE(j+3);
    }
    ITER(8*j+6);
  }
  float l=o[8];
  { auto rr=__builtin_amdgcn_permlane32_swap(__float_as_uint(l),__float_as_uint(l),false,false); l=__uint_as_float(rr[0]); }
  bool bad = !(l<1e30f);
  #pragma unroll
  for(int i=0;i<8;i++) bad = bad || !(fabsf(o[i])<1e37f);
  if(__builtin_expect(__any(bad),0)){
    const u16* kgl=Ks+(long)h*S*DH+lane*8;
    const u16* vgl=Vt+(long)h*S*DH+hh*128+(r&15)*8;
    float m=-INFINITY; f32x16 z={};
    #pragma unroll
    for(int i=0;i<16;i++) o[i]=0.f;
    for(int t=0;t<64;t++){
      bf16x8 kf=*(const bf16x8*)(kgl+t*512);
      bf16x8 v0=*(const bf16x8*)(vgl+t*512), v1=*(const bf16x8*)(vgl+t*512+256);
      if(cl){ u32x4 one={0x3F803F80u,0x3F803F80u,0x3F803F80u,0x3F803F80u}; v0=__builtin_bit_cast(bf16x8,one); v1=v0; }
      f32x16 s0=MFMA32(kf,qf,z);
      float tm=s0[0];
      #pragma unroll
      for(int i=1;i<16;i++) tm=fmaxf(tm,s0[i]);
      auto rr=__builtin_amdgcn_permlane32_swap(__float_as_uint(tm),__float_as_uint(tm),false,false);
      tm=fmaxf(__uint_as_float(rr[0]),__uint_as_float(rr[1]));
      float mn=fmaxf(m,tm);
      float alpha=EX(m-mn); m=mn;
      #pragma unroll
      for(int i=0;i<16;i++){ o[i]*=alpha; s0[i]-=mn; }
      pv_step(s0,v0,v1,o);
    }
    l=o[8];
    auto rr=__builtin_amdgcn_permlane32_swap(__float_as_uint(l),__float_as_uint(l),false,false); l=__uint_as_float(rr[0]);
  }
  float rl=1.0f/l;
  u32x4 w={pk2(o[0]*rl,o[1]*rl),pk2(o[2]*rl,o[3]*rl),pk2(o[4]*rl,o[5]*rl),pk2(o[6]*rl,o[7]*rl)};
  *(u32x4*)(O+(long)((qb*8+wid)*32+r)*D+h*DH+hh*8)=w;
}
DI void down_role(char* lds, const u16* __restrict__ x, const u16* __restrict__ hb, const u16* __restrict__ wd, float* __restrict__ yb, int mt, int nt){
  const int tid=threadIdx.x, lane=tid&63; const int wid=__builtin_amdgcn_readfirstlane(tid>>6);
  const unsigned lds0=(unsigned)(uintptr_t)lds;
  __builtin_amdgcn_s_setprio(GU_PRIO);
  for(int i=0;i<GU_DELAY;i++) __builtin_amdgcn_s_sleep(16);
  const int rr=lane>>3, sl=lane&7;
  const int prow=wid*8+rr;
  const u16* hs=hb+(long)(mt*64+prow)*HP+((sl^((prow>>1)&7))*8);
  const int wc=nt*64+(((sl>>2)^((prow>>1)&1))*32)+(sl&3)*8;
  auto issue=[&](int c){ const unsigned b_=lds0+(c&3)*16384+wid*1024; int k=c*64+prow; k=k>F-1?F-1:k;
    glds16(hs+c*64,b_); glds16(wd+(long)k*D+wc,b_+8192); };
  issue(0); issue(1); issue(2);
  const int wm=wid&1, wn=(wid>>1)&1, kh=wid>>2; const int r=lane&31, hh=lane>>5;
  const int xr=wm*32+r; const int xsw=(xr>>1)&7;
  lds_cptr xa0=(lds_cptr)lds+xr*128+(((4*kh+hh)^xsw)*16);
  lds_cptr xa1=(lds_cptr)lds+xr*128+(((4*kh+2+hh)^xsw)*16);
  const int blk=(lane>>4)&1, q4=(lane&15)>>2, p4=lane&3;
  lds_cptr wa=(lds_cptr)lds+8192+(32*kh+4*hh+q4)*128+((wn^((q4>>1)&1))*64)+blk*32+p4*8;
  f32x16 acc={};
  #pragma unroll 1
  for(int c=0;c<22;c++){
    if(c<20) asm volatile("s_waitcnt vmcnt(4) lgkmcnt(0)\n\ts_barrier":::"memory");
    else asm volatile("s_waitcnt vmcnt(0) lgkmcnt(0)\n\ts_barrier":::"memory");
    const int sb=(c&3)*16384;
    bf16x8 x0=lds_ld(xa0+sb), x1=lds_ld(xa1+sb);
    bf16x8 w0=cat8(lds_tr(wa+sb), lds_tr(wa+sb+1024));
    bf16x8 w1=cat8(lds_tr(wa+sb+2048), lds_tr(wa+sb+2048+1024));
    acc=MFMA32(w0,x0,acc); acc=MFMA32(w1,x1,acc);
    SB();
    if(c+3<22) issue(c+3);
  }
  asm volatile("s_waitcnt lgkmcnt(0)\n\ts_barrier":::"memory");
  float* xch=(float*)lds+(wid&3)*1024+lane;
  if(kh==1){
    #pragma unroll
    for(int i=0;i<16;i++) xch[i*64]=acc[i];
  }
  __syncthreads();
  if(kh==1) return;
  const long ro=(long)(mt*64+xr)*D+nt*64+wn*32+4*hh;
  #pragma unroll
  for(int q=0;q<4;q++){
    u32x2 xv=*(const u32x2*)(x+ro+8*q);
    f32x4 v={acc[4*q]+xch[(4*q)*64]+lo16f(xv[0]),acc[4*q+1]+xch[(4*q+1)*64]+hi16f(xv[0]),acc[4*q+2]+xch[(4*q+2)*64]+lo16f(xv[1]),acc[4*q+3]+xch[(4*q+3)*64]+hi16f(xv[1])};
    *(f32x4*)(yb+ro+8*q)=v;
  }
}
__global__ __launch_bounds__(512,4) void k2_fused(const u16* __restrict__ Qs, const u16* __restrict__ Ks, const u16* __restrict__ Vt, u16* __restrict__ O,
     const u16* __restrict__ hb, const u16* __restrict__ x, const u16* __restrict__ wd, float* __restrict__ yb){
  extern __shared__ __attribute__((aligned(16))) char lds[];
  const int b=blockIdx.x;
  if(b<256) attn_role(lds,Qs,Ks,Vt,O,b);
  else { const int u=b-256; down_role(lds,x,hb,wd,yb,(u&7)*4+((u>>3)&3),u>>5); }
}

extern "C" void kernel_launch(void* const* d_in, const int* in_sizes, int n_in, void* d_out, int out_size, void* d_ws, size_t ws_size, hipStream_t stream){
  const u16* x=(const u16*)d_in[0]; const u16* wqkv=(const u16*)d_in[1]; const u16* wo=(const u16*)d_in[2];
  const u16* wg=(const u16*)d_in[4]; const u16* wu=(const u16*)d_in[5]; const u16* wd=(const u16*)d_in[6];
  char* ws=(char*)d_ws;
  unsigned* ctl=(unsigned*)ws; ws+=4096;
  u16* xn=(u16*)ws; ws+=S*D*2;
  u16* hb=(u16*)ws; ws+=(size_t)S*HP*2;
  u16* o=(u16*)ws; ws+=(size_t)S*D*2;
  u16* Qs=(u16*)ws; ws+=(size_t)S*D*2;
  u16* Ks=(u16*)ws; ws+=(size_t)S*D*2;
  u16* Vt=(u16*)ws; ws+=(size_t)S*D*2;
  float* yb=(float*)ws; ws+=(size_t)S*D*4;
  (void)hipFuncSetAttribute((const void*)k2_fused, hipFuncAttributeMaxDynamicSharedMemorySize, K2_LDS);
  (void)hipFuncSetAttribute((const void*)k1_qkv, hipFuncAttributeMaxDynamicSharedMemorySize, K1_LDS);
  (void)hipFuncSetAttribute((const void*)k3_out, hipFuncAttributeMaxDynamicSharedMemorySize, K3_LDS);
  k1_qkv<<<256,512,K1_LDS,stream>>>(x,wqkv,Qs,Ks,Vt,wg,wu,hb);
  k2_fused<<<512,512,K2_LDS,stream>>>(Qs,Ks,Vt,o,hb,x,wd,yb);
  k3_out<<<256,512,K3_LDS,stream>>>(yb,o,wo,(u16*)d_out);
}
```

```cpp
#define K1MAP 0
#define GU_DELAY 3
#define ABL 0
#define NBLK (256+352)
#define PACE 0
#define BARMODE 0
#define FIRSTW "0"
#include <hip/hip_runtime.h>
#include <hip/hip_bf16.h>
#include <stdint.h>

typedef unsigned short u16;
typedef __attribute__((ext_vector_type(8))) __bf16 bf16x8;
typedef __attribute__((ext_vector_type(2))) __bf16 bf16x2;
typedef __attribute__((ext_vector_type(2))) float f32x2;
typedef __attribute__((ext_vector_type(16))) float f32x16;
typedef __attribute__((ext_vector_type(4))) unsigned u32x4;
typedef __attribute__((ext_vector_type(2))) unsigned u32x2;
#define DI __device__ __forceinline__
#define MFMA32(a,b,c) __builtin_amdgcn_mfma_f32_32x32x16_bf16((a),(b),(c),0,0,0)

DI float bf2f(u16 b){ return __uint_as_float(((unsigned)b)<<16); }
DI u16 f2bf(float f){ unsigned u=__float_as_uint(f); u += 0x7fffu + ((u>>16)&1u); return (u16)(u>>16); }
DI float rbf(float f){ return bf2f(f2bf(f)); }
DI unsigned pk2(float lo, float hi){ f32x2 v={lo,hi}; bf16x2 b=__builtin_convertvector(v,bf16x2); return __builtin_bit_cast(unsigned,b); }

constexpr int S=2048, D=512, H=32, DH=16, F=1376;
constexpr float QSCALE = 0.25f*1.4426950408889634f;

DI void glds16(const void* g, unsigned lds_base) {
  unsigned sv; asm volatile("s_mov_b32 %0, m0\n\ts_mov_b32 m0, %2\n\ts_nop 0\n\tglobal_load_lds_dwordx4 %1, off\n\ts_mov_b32 m0, %0" : "=&s"(sv) : "v"(g), "s"(lds_base) : "memory"); }

typedef __attribute__((address_space(3))) const char* lds_cptr;
DI bf16x8 lds_ld(lds_cptr p){ return *(const __attribute__((address_space(3))) bf16x8*)p; }

#define SB() __builtin_amdgcn_sched_barrier(0)
typedef __attribute__((ext_vector_type(4))) float f32x4;
typedef __attribute__((ext_vector_type(4))) short s16x4;
typedef short v4i16_t __attribute__((ext_vector_type(4)));
#define MFMA16(a,b,c) __builtin_amdgcn_mfma_f32_16x16x32_bf16((a),(b),(c),0,0,0)
DI s16x4 lds_tr(lds_cptr p){ return __builtin_bit_cast(s16x4, __builtin_amdgcn_ds_read_tr16_b64_v4i16((__attribute__((address_space(3))) v4i16_t*)p)); }
DI bf16x8 cat8(s16x4 a, s16x4 b){ return __builtin_bit_cast(bf16x8, __builtin_shufflevector(a,b,0,1,2,3,4,5,6,7)); }
DI float dpp_add(float v, const int ctrl_sel){
  int t;
  if(ctrl_sel==0) t=__builtin_amdgcn_mov_dpp(__float_as_int(v),0xB1,0xF,0xF,true);
  else if(ctrl_sel==1) t=__builtin_amdgcn_mov_dpp(__float_as_int(v),0x4E,0xF,0xF,true);
  else t=__builtin_amdgcn_mov_dpp(__float_as_int(v),0x141,0xF,0xF,true);
  return v+__int_as_float(t);
}
DI float lo16f(unsigned u){ return __uint_as_float(u<<16); }
DI float hi16f(unsigned u){ return __uint_as_float(u&0xffff0000u); }
DI float dot2bf(unsigned a, unsigned b, float c){
  return __builtin_amdgcn_fdot2_f32_bf16(__builtin_bit_cast(bf16x2,a),__builtin_bit_cast(bf16x2,b),c,false); }

#define TL_BEGIN()
#define TL_END(k)
constexpr int HP=1408;
constexpr int K1_WST=24576, K1_NST=4, K1_X=K1_WST*K1_NST, K1_LDS=K1_X+8*8192;

template<int NT> DI void gu_stage(lds_cptr xa, lds_cptr wa, int xo0, int xo1, f32x16 (&ag)[2], f32x16 (&au)[2]){
  #pragma unroll
  for(int s=0;s<2;s++){
    bf16x8 gf=cat8(lds_tr(wa+s*6144), lds_tr(wa+s*6144+1536));
    bf16x8 uf=cat8(lds_tr(wa+12288+s*6144), lds_tr(wa+12288+s*6144+1536));
    #pragma unroll
    for(int t=0;t<NT;t++){
      bf16x8 xf=lds_ld(xa+t*4096+(s?xo1:xo0));
      ag[t]=MFMA32(gf,xf,ag[t]); au[t]=MFMA32(uf,xf,au[t]);
    }
  }
}
DI void gu_store(const f32x16& ag, const f32x16& au, u16* hp, bool zero){
  u32x4 o0,o1;
  #pragma unroll
  for(int i=0;i<8;i++){
    float hv[2];
    #pragma unroll
    for(int e=0;e<2;e++){ float g=ag[2*i+e]; float sg=g*__builtin_amdgcn_rcpf(1.0f+__builtin_amdgcn_exp2f(-1.4426950408889634f*g)); hv[e]=zero?0.f:sg*au[2*i+e]; }
    unsigned w=pk2(hv[0],hv[1]); if(i<4) o0[i]=w; else o1[i-4]=w;
  }
  *(u32x4*)hp=o0; *(u32x4*)(hp+16)=o1;
}

__global__ __launch_bounds__(512,2) void k1_qkv(const u16* __restrict__ x, const u16* __restrict__ wqkv,
     u16* __restrict__ Qs, u16* __restrict__ Ks, u16* __restrict__ Vt,
     const u16* __restrict__ wg, const u16* __restrict__ wu, u16* __restrict__ hb, unsigned* __restrict__ ctl){
  extern __shared__ __attribute__((aligned(16))) char lds[];
  const int tid=threadIdx.x, lane=tid&63; const int wid=__builtin_amdgcn_readfirstlane(tid>>6);
  const int b=blockIdx.x;
  const int rb=(b&7)*4+((b>>3)&3), hg=b>>5;
  const unsigned lds0=(unsigned)(uintptr_t)lds;
  const int rr=lane>>3, p=lane&7;
  const int lrow=wid*8+rr;
  TL_BEGIN();
  const u16* xp=x+(long)(rb*64+lrow)*D+p*8;
  u32x4 xv[8];
  #pragma unroll
  for(int i=0;i<8;i++) asm volatile("global_load_dwordx4 %0, %1, off offset:%2" : "=v"(xv[i]) : "v"(xp), "n"(i*128) : "memory");
  const u16* wp=wqkv+(long)lrow*(3*D)+hg*64+(((p>>1)^(((lrow>>1)&1)|(((lrow>>3)&1)<<1)))*16)+(p&1)*8;
  asm volatile("s_barrier":::"memory");
  #pragma unroll
  for(int st=0;st<K1_NST;st++){
    #pragma unroll
    for(int ty=0;ty<3;ty++) glds16(wp+(long)st*64*(3*D)+ty*D, lds0+st*K1_WST+ty*8192+wid*1024);
  }
  const int ft0=hg*5+(hg<3?hg:3);
  int goff[3];
  #pragma unroll
  for(int i=0;i<3;i++){
    const int pc=wid*3+i; const int i16=(pc%12)*64+lane; const int k=i16/24, ch=i16%24;
    int col=ft0*32+(((ch>>2)^((k>>1)&1))*32)+(ch&3)*8; col=col>F-8?F-8:col;
    goff[i]=k*F+col;
  }
  const u16* gbase=(wid<4)?wg:wu;
#define GU_ISS(g_) do{ _Pragma("unroll") for(int i=0;i<3;i++) glds16(gbase+(long)(g_)*32*F+goff[i], lds0+((g_)&3)*K1_WST+(wid*3+i)*1024); }while(0)
  asm volatile("s_waitcnt vmcnt(12)" : "+v"(xv[0]),"+v"(xv[1]),"+v"(xv[2]),"+v"(xv[3]),"+v"(xv[4]),"+v"(xv[5]),"+v"(xv[6]),"+v"(xv[7]) :: "memory");
  float s1=0.f, s2=0.f;
  #pragma unroll
  for(int i=0;i<8;i++){
    #pragma unroll
    for(int j=0;j<4;j++){ s1=dot2bf(xv[i][j],0x3F803F80u,s1); s2=dot2bf(xv[i][j],xv[i][j],s2); }
  }
  s1=dpp_add(s1,0); s2=dpp_add(s2,0); s1=dpp_add(s1,1); s2=dpp_add(s2,1); s1=dpp_add(s1,2); s2=dpp_add(s2,2);
  const float mean=s1*(1.0f/D);
  float var=s2*(1.0f/D)-mean*mean;
  if(__builtin_expect(__any(!(mean*mean<=16.f*var)),0)){
    float q2=0.f;
    #pragma unroll
    for(int i=0;i<8;i++){
      #pragma unroll
      for(int j=0;j<4;j++){ float a=lo16f(xv[i][j])-mean, c=hi16f(xv[i][j])-mean; q2=fmaf(a,a,q2); q2=fmaf(c,c,q2); }
    }
    q2=dpp_add(q2,0); q2=dpp_add(q2,1); q2=dpp_add(q2,2);
    var=q2*(1.0f/D);
  }
  const float mb=rbf(mean);
  const float den=rbf(sqrtf(rbf(rbf(var)+1e-5f)));
  const float rinv=1.0f/den;
  char* xw=lds+K1_X+lrow*128+((p^((lrow>>1)&7))*16);
#define NRM1(i,j) do{ unsigned c_=pk2(lo16f(xv[i][j])-mb, hi16f(xv[i][j])-mb); xv[i][j]=pk2(lo16f(c_)*rinv, hi16f(c_)*rinv); }while(0)
#define NORM(i) do{ NRM1(i,0); NRM1(i,1); NRM1(i,2); NRM1(i,3); *(u32x4*)(xw+(i)*8192)=xv[i]; }while(0)
  NORM(0); NORM(1);
  const int rt2=wid&1, hd=wid>>1;
  const int g=lane>>4, r16=lane&15;
  const int xsw=(r16>>1)&7;
  lds_cptr xa0=(lds_cptr)lds+K1_X+(rt2*32+r16)*128+((g^xsw)*16);
  lds_cptr xa1=(lds_cptr)lds+K1_X+(rt2*32+r16)*128+(((4+g)^xsw)*16);
  const int q4=(lane&15)>>2, p4=lane&3;
  const int fk=((q4>>1)&1)|((g&1)<<1);
  lds_cptr wa=(lds_cptr)lds+(8*g+q4)*128+((hd^fk)*32)+p4*8;
  f32x4 aq[2]={}, ak[2]={}, av[2]={};
  bf16x8 fx[2][2][2], fw[2][2][3];
#define TRW(set,s,ty,c) fw[set][s][ty]=cat8(lds_tr(wa+((c)%K1_NST)*K1_WST+(ty)*8192+(s)*4096), lds_tr(wa+((c)%K1_NST)*K1_WST+(ty)*8192+(s)*4096+512))
  asm volatile("s_waitcnt vmcnt(9) lgkmcnt(0)\n\ts_barrier":::"memory");
  fx[0][0][0]=lds_ld(xa0); fx[0][0][1]=lds_ld(xa0+2048); fx[0][1][0]=lds_ld(xa1); fx[0][1][1]=lds_ld(xa1+2048);
  TRW(0,0,0,0); TRW(0,0,1,0); TRW(0,0,2,0); TRW(0,1,0,0); TRW(0,1,1,0); TRW(0,1,2,0);
#define K1_ISS1(c,i) do{ if((c)<4) glds16(wp+(long)((c)+4)*64*(3*D)+(i)*D, lds0+(c)*K1_WST+(i)*8192+wid*1024); \
                        else if((c)<7) glds16(gbase+(long)((c)-4)*32*F+goff[i], lds0+((c)-4)*K1_WST+(wid*3+(i))*1024); }while(0)
  #pragma unroll
  for(int c=0;c<8;c++){
    const int cs=c&1, ns=cs^1; const int xb=(c+1)*8192; const int ni=(c+2)&7;
    if(c<7) asm volatile("s_waitcnt vmcnt(6) lgkmcnt(0)\n\ts_barrier":::"memory");
    if(c<7){ fx[ns][0][0]=lds_ld(xa0+xb); fx[ns][0][1]=lds_ld(xa0+xb+2048); TRW(ns,0,0,c+1); }
    aq[0]=MFMA16(fw[cs][0][0],fx[cs][0][0],aq[0]); aq[1]=MFMA16(fw[cs][0][0],fx[cs][0][1],aq[1]); ak[0]=MFMA16(fw[cs][0][1],fx[cs][0][0],ak[0]);
    if(c<6) NRM1(ni,0);
    K1_ISS1(c,0);
    SB();
    if(c<7){ TRW(ns,0,1,c+1); TRW(ns,0,2,c+1); }
    ak[1]=MFMA16(fw[cs][0][1],fx[cs][0][1],ak[1]); av[0]=MFMA16(fx[cs][0][0],fw[cs][0][2],av[0]); av[1]=MFMA16(fx[cs][0][1],fw[cs][0][2],av[1]);
    if(c<6) NRM1(ni,1);
    K1_ISS1(c,1);
    SB();
    if(c<7){ fx[ns][1][0]=lds_ld(xa1+xb); fx[ns][1][1]=lds_ld(xa1+xb+2048); TRW(ns,1,0,c+1); }
    aq[0]=MFMA16(fw[cs][1][0],fx[cs][1][0],aq[0]); aq[1]=MFMA16(fw[cs][1][0],fx[cs][1][1],aq[1]); ak[0]=MFMA16(fw[cs][1][1],fx[cs][1][0],ak[0]);
    if(c<6) NRM1(ni,2);
    K1_ISS1(c,2);
    SB();
    if(c<7){ TRW(ns,1,1,c+1); TRW(ns,1,2,c+1); }
    ak[1]=MFMA16(fw[cs][1][1],fx[cs][1][1],ak[1]); av[0]=MFMA16(fx[cs][1][0],fw[cs][1][2],av[0]); av[1]=MFMA16(fx[cs][1][1],fw[cs][1][2],av[1]);
    if(c<6){ NRM1(ni,3); *(u32x4*)(xw+ni*8192)=xv[ni]; }
    SB();
  }
  {
    const int h=hg*4+hd; const int T=rb*2+rt2;
    #pragma unroll
    for(int tt=0;tt<2;tt++){
      const long qo=(long)h*S*DH+T*512+(g>>1)*256+(tt*16+r16)*8+(g&1)*4;
      u32x2 wq={pk2(aq[tt][0]*QSCALE,aq[tt][1]*QSCALE),pk2(aq[tt][2]*QSCALE,aq[tt][3]*QSCALE)};
      u32x2 wk={pk2(ak[tt][0],ak[tt][1]),pk2(ak[tt][2],ak[tt][3])};
      const long vo=(long)h*S*DH+(2*T+tt)*256+(g&1)*128+r16*8+(g>>1)*4;
      u32x2 wv={pk2(av[tt][0],av[tt][1]),pk2(av[tt][2],av[tt][3])};
      asm volatile("global_store_dwordx2 %0, %1, off\n\tglobal_store_dwordx2 %2, %3, off\n\tglobal_store_dwordx2 %4, %5, off"
         :: "v"(Qs+qo),"v"(wq),"v"(Ks+qo),"v"(wk),"v"(Vt+vo),"v"(wv) : "memory");
    }
  }
  {
    const int r=lane&31, hh=lane>>5;
    const int ftl = wid<4 ? wid : 4+((wid-4)>>1);
    const int tt0 = wid<4 ? 0 : ((wid-4)&1);
    const int xsw2=(r>>1)&7;
    lds_cptr xg=(lds_cptr)lds+K1_X+(tt0*32+r)*128;
    const int blk=(lane>>4)&1;
    lds_cptr wg_=(lds_cptr)lds+(8*hh+q4)*384+((ftl^((q4>>1)&1))*64)+blk*32+p4*8;
    f32x16 ag[2]={}, au[2]={};
    bf16x8 gf[2][2], uf[2][2], xf[2][2][2];
    const bool two=(wid<4);
#define GRD(gs_,s) do{ const int st_=(gs_)&1; lds_cptr w_=wg_+((gs_)&3)*K1_WST+(s)*6144; \
      gf[st_][s]=cat8(lds_tr(w_), lds_tr(w_+1536)); uf[st_][s]=cat8(lds_tr(w_+12288), lds_tr(w_+12288+1536)); \
      lds_cptr x_=xg+((gs_)>>1)*8192+(((4*((gs_)&1)+2*(s)+hh)^xsw2)*16); xf[st_][s][0]=lds_ld(x_); if(two) xf[st_][s][1]=lds_ld(x_+4096); }while(0)
#define GMM(gs_,s) do{ const int st_=(gs_)&1; ag[0]=MFMA32(gf[st_][s],xf[st_][s][0],ag[0]); au[0]=MFMA32(uf[st_][s],xf[st_][s][0],au[0]); \
      if(two){ ag[1]=MFMA32(gf[st_][s],xf[st_][s][1],ag[1]); au[1]=MFMA32(uf[st_][s],xf[st_][s][1],au[1]); } }while(0)
#define GU_ISS1(g_,i) glds16(gbase+(long)(g_)*32*F+goff[i], lds0+((g_)&3)*K1_WST+(wid*3+(i))*1024)
    asm volatile("s_waitcnt vmcnt(12) lgkmcnt(0)\n\ts_barrier":::"memory");
    GRD(0,0); GRD(0,1);
    GU_ISS(3);
    #pragma unroll
    for(int gs=0;gs<16;gs++){
      if(gs==0) asm volatile("s_waitcnt vmcnt(12) lgkmcnt(0)\n\ts_barrier":::"memory");
      else if(gs==1) asm volatile("s_waitcnt vmcnt(12) lgkmcnt(0)\n\ts_barrier":::"memory");
      else if(gs<13) asm volatile("s_waitcnt vmcnt(6) lgkmcnt(0)\n\ts_barrier":::"memory");
      else if(gs==13) asm volatile("s_waitcnt vmcnt(3) lgkmcnt(0)\n\ts_barrier":::"memory");
      else if(gs==14) asm volatile("s_waitcnt vmcnt(0) lgkmcnt(0)\n\ts_barrier":::"memory");
      if(gs<15) GRD(gs+1,0);
      GMM(gs,0);
      if(gs+4<16){ GU_ISS1(gs+4,0); GU_ISS1(gs+4,1); }
      SB();
      if(gs<15) GRD(gs+1,1);
      GMM(gs,1);
      if(gs+4<16){ GU_ISS1(gs+4,2); }
      SB();
    }
    const int ft=ft0+ftl;
    const bool own=(ftl<5)||(hg<3)||(hg==7);
    if(own){
      u16* hp=hb+(long)(rb*64+tt0*32+r)*HP+ft*32+hh*8;
      gu_store(ag[0],au[0],hp,ft>=43);
      if(wid<4) gu_store(ag[1],au[1],hp+(long)32*HP,false);
    }
  }
  if(b==0) ctl[tid]=0u;
  TL_END(0);
}

#define GU_PRIO 2
constexpr int A2_K=0, A2_V=32768, A2_C=65536, K2_LDS=65536+4096;
#define EX(x) __builtin_amdgcn_exp2f(x)
DI void pv_step(const f32x16& sc, bf16x8 v0, bf16x8 v1, f32x16& o){
  u32x4 p0, p1;
  #pragma unroll
  for(int i=0;i<4;i++){ p0[i]=pk2(EX(sc[2*i]),EX(sc[2*i+1])); }
  #pragma unroll
  for(int i=0;i<4;i++){ p1[i]=pk2(EX(sc[8+2*i]),EX(sc[8+2*i+1])); }
  o=MFMA32(v0,__builtin_bit_cast(bf16x8,p0),o);
  o=MFMA32(v1,__builtin_bit_cast(bf16x8,p1),o);
}
DI void attn_role(char* lds, const u16* __restrict__ Qs, const u16* __restrict__ Ks, const u16* __restrict__ Vt, u16* O, int b, unsigned* ctl){
  const int tid=threadIdx.x, lane=tid&63; const int wid=__builtin_amdgcn_readfirstlane(tid>>6);
  const int h=(b&7)*4+((b>>3)>>3), qb=(b>>3)&7;
  const int r=lane&31, hh=lane>>5;
  const unsigned lds0=(unsigned)(uintptr_t)lds;
  const u16* qp=Qs+(long)h*S*DH+(qb*8+wid)*512+lane*8;
  bf16x8 qf;
  asm volatile("global_load_dwordx4 %0, %1, off" : "=v"(qf) : "v"(qp) : "memory");
  const u16* kg=Ks+(long)h*S*DH+wid*512+lane*8;
  const u16* vg=Vt+(long)h*S*DH+wid*512+lane*8;
#define A_ISSUE(c) do{ glds16(kg+(c)*4096, lds0+A2_K+((c)&3)*8192+wid*1024); glds16(vg+(c)*4096, lds0+A2_V+((c)&3)*8192+wid*1024); }while(0)
  A_ISSUE(0); A_ISSUE(1); A_ISSUE(2); A_ISSUE(3);
  { u32x2 v; v[0]=((tid&31)<2)?0x3F803F80u:0u; v[1]=v[0]; *(u32x2*)(lds+A2_C+tid*8)=v; }
  lds_cptr kp=(lds_cptr)lds+A2_K+lane*16;
  const bool cl=(r>=16);
  lds_cptr vp=(lds_cptr)lds+(cl ? A2_C : A2_V)+hh*256+(r&15)*16;
  const int vinc= cl?0:1024; const int vwrap= cl?0:(1024-32768);
  asm volatile("s_waitcnt vmcnt(6) lgkmcnt(0)\n\ts_barrier": "+v"(qf) ::"memory");
  f32x16 negm; f32x16 sA, sB;
  bf16x8 kA, kB, vA0, vA1, vB0, vB1;
  {
    f32x16 z={}; bf16x8 k0=lds_ld(kp);
    kA=lds_ld(kp+1024); vA0=lds_ld(vp); vA1=lds_ld(vp+512); vp+=vinc;
    f32x16 s0=MFMA32(k0,qf,z);
    float m=s0[0];
    #pragma unroll
    for(int i=1;i<16;i++) m=fmaxf(m,s0[i]);
    auto rr=__builtin_amdgcn_permlane32_swap(__float_as_uint(m),__float_as_uint(m),false,false);
    m=fmaxf(__uint_as_float(rr[0]),__uint_as_float(rr[1]));
    float nm=-(m+60.f);
    #pragma unroll
    for(int i=0;i<16;i++){ negm[i]=nm; sA[i]=s0[i]+nm; }
  }
  f32x16 o={};
#define STEP(sC,sN,kU,kL,vU0,vU1,vL0,vL1,t) do{ \
    u32x4 p0,p1; \
    kL=lds_ld(kp+(((t)+2)&31)*1024); vL0=lds_ld(vp); vL1=lds_ld(vp+512); vp+=((((t)+1)&31)==31)?vwrap:vinc; \
    float e0=EX(sC[0]),e1=EX(sC[1]),e2=EX(sC[2]),e3=EX(sC[3]); SB(); \
    sN=MFMA32(kU,qf,negm); SB(); \
    float e4=EX(sC[4]),e5=EX(sC[5]),e6=EX(sC[6]),e7=EX(sC[7]); \
    p0[0]=pk2(e0,e1); p0[1]=pk2(e2,e3); p0[2]=pk2(e4,e5); p0[3]=pk2(e6,e7); SB(); \
    float e8=EX(sC[8]),e9=EX(sC[9]); SB(); \
    o=MFMA32(vU0,__builtin_bit_cast(bf16x8,p0),o); SB(); \
    float e10=EX(sC[10]),e11=EX(sC[11]),e12=EX(sC[12]),e13=EX(sC[13]),e14=EX(sC[14]),e15=EX(sC[15]); \
    p1[0]=pk2(e8,e9); p1[1]=pk2(e10,e11); p1[2]=pk2(e12,e13); p1[3]=pk2(e14,e15); SB(); \
    o=MFMA32(vU1,__builtin_bit_cast(bf16x8,p1),o); SB(); \
  }while(0)
#define ITER(t) do{ STEP(sA,sB,kA,kB,vA0,vA1,vB0,vB1,(t)); STEP(sB,sA,kB,kA,vB0,vB1,vA0,vA1,(t)+1); }while(0)
  #pragma unroll
  for(int j=0;j<8;j++){
    ITER(8*j); ITER(8*j+2); ITER(8*j+4);
    if(j<7){
      if(j==0) asm volatile("s_waitcnt vmcnt(4) lgkmcnt(0)\n\ts_barrier":::"memory");
      else if(j<6) asm volatile("s_waitcnt vmcnt(2) lgkmcnt(0)\n\ts_barrier":::"memory");
      else asm volatile("s_waitcnt vmcnt(0) lgkmcnt(0)\n\ts_barrier":::"memory");
      if(j>=1 && j+3<8) A_ISSUE(j+3);
    }
    ITER(8*j+6);
  }
  float l=o[8];
  { auto rr=__builtin_amdgcn_permlane32_swap(__float_as_uint(l),__float_as_uint(l),false,false); l=__uint_as_float(rr[0]); }
  bool bad = !(l<1e30f);
  #pragma unroll
  for(int i=0;i<8;i++) bad = bad || !(fabsf(o[i])<1e37f);
  if(__builtin_expect(__any(bad),0)){
    const u16* kgl=Ks+(long)h*S*DH+lane*8;
    const u16* vgl=Vt+(long)h*S*DH+hh*128+(r&15)*8;
    float m=-INFINITY; f32x16 z={};
    #pragma unroll
    for(int i=0;i<16;i++) o[i]=0.f;
    for(int t=0;t<64;t++){
      bf16x8 kf=*(const bf16x8*)(kgl+t*512);
      bf16x8 v0=*(const bf16x8*)(vgl+t*512), v1=*(const bf16x8*)(vgl+t*512+256);
      if(cl){ u32x4 one={0x3F803F80u,0x3F803F80u,0x3F803F80u,0x3F803F80u}; v0=__builtin_bit_cast(bf16x8,one); v1=v0; }
      f32x16 s0=MFMA32(kf,qf,z);
      float tm=s0[0];
      #pragma unroll
      for(int i=1;i<16;i++) tm=fmaxf(tm,s0[i]);
      auto rr=__builtin_amdgcn_permlane32_swap(__float_as_uint(tm),__float_as_uint(tm),false,false);
      tm=fmaxf(__uint_as_float(rr[0]),__uint_as_float(rr[1]));
      float mn=fmaxf(m,tm);
      float alpha=EX(m-mn); m=mn;
      #pragma unroll
      for(int i=0;i<16;i++){ o[i]*=alpha; s0[i]-=mn; }
      pv_step(s0,v0,v1,o);
    }
    l=o[8];
    auto rr=__builtin_amdgcn_permlane32_swap(__float_as_uint(l),__float_as_uint(l),false,false); l=__uint_as_float(rr[0]);
  }
  float rl=1.0f/l;
  u32x4 w={pk2(o[0]*rl,o[1]*rl),pk2(o[2]*rl,o[3]*rl),pk2(o[4]*rl,o[5]*rl),pk2(o[6]*rl,o[7]*rl)};
  asm volatile("global_store_dwordx4 %0, %1, off sc1\n\ts_waitcnt vmcnt(0)\n\ts_barrier" :: "v"(O+(long)((qb*8+wid)*32+r)*D+h*DH+hh*8), "v"(w) : "memory");
  if(tid==0) __hip_atomic_fetch_add(ctl+qb*32,1u,__ATOMIC_RELAXED,__HIP_MEMORY_SCOPE_AGENT);
}
DI void down_role(char* lds, const u16* __restrict__ x, const u16* __restrict__ hb, const u16* __restrict__ wd, const u16* ob, const u16* __restrict__ wo, u16* __restrict__ out, unsigned* ctl, int mt, int nt){
  const int tid=threadIdx.x, lane=tid&63; const int wid=__builtin_amdgcn_readfirstlane(tid>>6);
  const unsigned lds0=(unsigned)(uintptr_t)lds;
  __builtin_amdgcn_s_setprio(GU_PRIO);
  for(int i=0;i<GU_DELAY;i++) __builtin_amdgcn_s_sleep(16);
  const int rr=lane>>3, sl=lane&7;
  const int prow=wid*8+rr;
  const u16* hs=hb+(long)(mt*64+prow)*HP+((sl^((prow>>1)&7))*8);
  const int wc=nt*64+(((sl>>2)^((prow>>1)&1))*32)+(sl&3)*8;
  auto issue=[&](int c){ const unsigned b_=lds0+(c&3)*16384+wid*1024; int k=c*64+prow; k=k>F-1?F-1:k;
    glds16(hs+c*64,b_); glds16(wd+(long)k*D+wc,b_+8192); };
  issue(0); issue(1); issue(2);
  const int wm=wid&1, wn=(wid>>1)&1, kh=wid>>2; const int r=lane&31, hh=lane>>5;
  const int xr=wm*32+r; const int xsw=(xr>>1)&7;
  lds_cptr xa0=(lds_cptr)lds+xr*128+(((4*kh+hh)^xsw)*16);
  lds_cptr xa1=(lds_cptr)lds+xr*128+(((4*kh+2+hh)^xsw)*16);
  const int blk=(lane>>4)&1, q4=(lane&15)>>2, p4=lane&3;
  lds_cptr wa=(lds_cptr)lds+8192+(32*kh+4*hh+q4)*128+((wn^((q4>>1)&1))*64)+blk*32+p4*8;
  f32x16 acc={};
  #pragma unroll 1
  for(int c=0;c<22;c++){
    if(c<20) asm volatile("s_waitcnt vmcnt(4) lgkmcnt(0)\n\ts_barrier":::"memory");
    else asm volatile("s_waitcnt vmcnt(0) lgkmcnt(0)\n\ts_barrier":::"memory");
    const int sb=(c&3)*16384;
    bf16x8 x0=lds_ld(xa0+sb), x1=lds_ld(xa1+sb);
    bf16x8 w0=cat8(lds_tr(wa+sb), lds_tr(wa+sb+1024));
    bf16x8 w1=cat8(lds_tr(wa+sb+2048), lds_tr(wa+sb+2048+1024));
    acc=MFMA32(w0,x0,acc); acc=MFMA32(w1,x1,acc);
    SB();
    if(c+3<22) issue(c+3);
  }
  asm volatile("s_waitcnt lgkmcnt(0)\n\ts_barrier":::"memory");
  { const u16* ws_=wo+(long)prow*D+wc;
    #pragma unroll
    for(int c=0;c<8;c++) glds16(ws_+(long)c*64*D, lds0+c*8192+wid*1024); }
  __builtin_amdgcn_s_setprio(0);
  if(tid==0){
    unsigned* cnt=ctl+(mt>>2)*32; unsigned spins=0;
    while(__hip_atomic_load(cnt,__ATOMIC_RELAXED,__HIP_MEMORY_SCOPE_AGENT)<32u){ __builtin_amdgcn_s_sleep(4); if(++spins>(1u<<24)) break; }
    __builtin_amdgcn_fence(__ATOMIC_ACQUIRE,"agent");
    asm volatile("s_waitcnt vmcnt(0)":::"memory");
  }
  asm volatile("s_barrier":::"memory");
  bf16x8 of[16];
  { const u16* op=ob+(long)(mt*64+xr)*D+(4*kh+hh)*8;
    #pragma unroll
    for(int c=0;c<8;c++){
      asm volatile("global_load_dwordx4 %0, %1, off offset:%2" : "=v"(of[2*c]) : "v"(op), "n"(c*128) : "memory");
      asm volatile("global_load_dwordx4 %0, %1, off offset:%2" : "=v"(of[2*c+1]) : "v"(op), "n"(c*128+32) : "memory");
    } }
  const long ro=(long)(mt*64+xr)*D+nt*64+wn*32+4*hh;
  u32x2 xr4[4];
  #pragma unroll
  for(int q=0;q<4;q++) asm volatile("global_load_dwordx2 %0, %1, off offset:%2" : "=v"(xr4[q]) : "v"(x+ro), "n"(q*16) : "memory");
  asm volatile("s_waitcnt vmcnt(0)\n\ts_barrier" : "+v"(of[0]),"+v"(of[1]),"+v"(of[2]),"+v"(of[3]),"+v"(of[4]),"+v"(of[5]),"+v"(of[6]),"+v"(of[7]),
      "+v"(of[8]),"+v"(of[9]),"+v"(of[10]),"+v"(of[11]),"+v"(of[12]),"+v"(of[13]),"+v"(of[14]),"+v"(of[15]),"+v"(xr4[0]),"+v"(xr4[1]),"+v"(xr4[2]),"+v"(xr4[3]) :: "memory");
  { lds_cptr wb=wa-8192;
    #pragma unroll
    for(int c=0;c<8;c++){
      bf16x8 w0=cat8(lds_tr(wb+c*8192), lds_tr(wb+c*8192+1024));
      bf16x8 w1=cat8(lds_tr(wb+c*8192+2048), lds_tr(wb+c*8192+2048+1024));
      acc=MFMA32(w0,of[2*c],acc); acc=MFMA32(w1,of[2*c+1],acc);
    } }
  asm volatile("s_waitcnt lgkmcnt(0)\n\ts_barrier":::"memory");
  float* xch=(float*)lds+(wid&3)*1024+lane;
  if(kh==1){
    #pragma unroll
    for(int i=0;i<16;i++) xch[i*64]=acc[i];
  }
  __syncthreads();
  if(kh==1) return;
  #pragma unroll
  for(int q=0;q<4;q++){
    float v0=acc[4*q]+xch[(4*q)*64]+lo16f(xr4[q][0]), v1=acc[4*q+1]+xch[(4*q+1)*64]+hi16f(xr4[q][0]);
    float v2=acc[4*q+2]+xch[(4*q+2)*64]+lo16f(xr4[q][1]), v3=acc[4*q+3]+xch[(4*q+3)*64]+hi16f(xr4[q][1]);
    u32x2 w={pk2(v0,v1),pk2(v2,v3)};
    *(u32x2*)(out+ro+8*q)=w;
  }
}
__global__ __launch_bounds__(512,4) void k2_fused(const u16* __restrict__ Qs, const u16* __restrict__ Ks, const u16* __restrict__ Vt, u16* O,
     const u16* __restrict__ hb, const u16* __restrict__ x, const u16* __restrict__ wd, const u16* __restrict__ wo, u16* __restrict__ out, unsigned* ctl){
  extern __shared__ __attribute__((aligned(16))) char lds[];
  const int b=blockIdx.x;
  TL_BEGIN();
  if(b<256) attn_role(lds,Qs,Ks,Vt,O,b,ctl);
  else { const int u=b-256; down_role(lds,x,hb,wd,O,wo,out,ctl,(u&7)*4+((u>>3)&3),u>>5); }
  TL_END(1);
}

extern "C" void kernel_launch(void* const* d_in, const int* in_sizes, int n_in, void* d_out, int out_size, void* d_ws, size_t ws_size, hipStream_t stream){
  const u16* x=(const u16*)d_in[0]; const u16* wqkv=(const u16*)d_in[1]; const u16* wo=(const u16*)d_in[2];
  const u16* wg=(const u16*)d_in[4]; const u16* wu=(const u16*)d_in[5]; const u16* wd=(const u16*)d_in[6];
  char* ws=(char*)d_ws;
  unsigned* ctl=(unsigned*)ws; ws+=4096;
  u16* xn=(u16*)ws; ws+=S*D*2;
  u16* hb=(u16*)ws; ws+=(size_t)S*HP*2;
  u16* o=(u16*)ws; ws+=(size_t)S*D*2;
  u16* Qs=(u16*)ws; ws+=(size_t)S*D*2;
  u16* Ks=(u16*)ws; ws+=(size_t)S*D*2;
  u16* Vt=(u16*)ws; ws+=(size_t)S*D*2;
  float* yb=(float*)ws; ws+=(size_t)S*D*4;
  (void)hipFuncSetAttribute((const void*)k2_fused, hipFuncAttributeMaxDynamicSharedMemorySize, K2_LDS);
  (void)hipFuncSetAttribute((const void*)k1_qkv, hipFuncAttributeMaxDynamicSharedMemorySize, K1_LDS);
  k1_qkv<<<256,512,K1_LDS,stream>>>(x,wqkv,Qs,Ks,Vt,wg,wu,hb,ctl);
  k2_fused<<<512,512,K2_LDS,stream>>>(Qs,Ks,Vt,o,hb,x,wd,wo,(u16*)d_out,ctl);
}
```

```cpp
#define GROT ((((b>>3)&3)*4+((b&7)>>1))&15)
#define POLL_SLEEP 4
#define K1MAP 0
#define GU_DELAY 3
#define ABL 0
#define NBLK (256+352)
#define PACE 0
#define BARMODE 0
#define FIRSTW "0"
#include <hip/hip_runtime.h>
#include <hip/hip_bf16.h>
#include <stdint.h>

typedef unsigned short u16;
typedef __attribute__((ext_vector_type(8))) __bf16 bf16x8;
typedef __attribute__((ext_vector_type(2))) __bf16 bf16x2;
typedef __attribute__((ext_vector_type(2))) float f32x2;
typedef __attribute__((ext_vector_type(16))) float f32x16;
typedef __attribute__((ext_vector_type(4))) unsigned u32x4;
typedef __attribute__((ext_vector_type(2))) unsigned u32x2;
#define DI __device__ __forceinline__
#define MFMA32(a,b,c) __builtin_amdgcn_mfma_f32_32x32x16_bf16((a),(b),(c),0,0,0)

DI float bf2f(u16 b){ return __uint_as_float(((unsigned)b)<<16); }
DI u16 f2bf(float f){ unsigned u=__float_as_uint(f); u += 0x7fffu + ((u>>16)&1u); return (u16)(u>>16); }
DI float rbf(float f){ return bf2f(f2bf(f)); }
DI unsigned pk2(float lo, float hi){ f32x2 v={lo,hi}; bf16x2 b=__builtin_convertvector(v,bf16x2); return __builtin_bit_cast(unsigned,b); }

constexpr int S=2048, D=512, H=32, DH=16, F=1376;
constexpr float QSCALE = 0.25f*1.4426950408889634f;

DI void glds16(const void* g, unsigned lds_base) {
  unsigned sv; asm volatile("s_mov_b32 %0, m0\n\ts_mov_b32 m0, %2\n\ts_nop 0\n\tglobal_load_lds_dwordx4 %1, off\n\ts_mov_b32 m0, %0" : "=&s"(sv) : "v"(g), "s"(lds_base) : "memory"); }

typedef __attribute__((address_space(3))) const char* lds_cptr;
DI bf16x8 lds_ld(lds_cptr p){ return *(const __attribute__((address_space(3))) bf16x8*)p; }

#define SB() __builtin_amdgcn_sched_barrier(0)
typedef __attribute__((ext_vector_type(4))) float f32x4;
typedef __attribute__((ext_vector_type(4))) short s16x4;
typedef short v4i16_t __attribute__((ext_vector_type(4)));
#define MFMA16(a,b,c) __builtin_amdgcn_mfma_f32_16x16x32_bf16((a),(b),(c),0,0,0)
DI s16x4 lds_tr(lds_cptr p){ return __builtin_bit_cast(s16x4, __builtin_amdgcn_ds_read_tr16_b64_v4i16((__attribute__((address_space(3))) v4i16_t*)p)); }
DI bf16x8 cat8(s16x4 a, s16x4 b){ return __builtin_bit_cast(bf16x8, __builtin_shufflevector(a,b,0,1,2,3,4,5,6,7)); }
DI float dpp_add(float v, const int ctrl_sel){
  int t;
  if(ctrl_sel==0) t=__builtin_amdgcn_mov_dpp(__float_as_int(v),0xB1,0xF,0xF,true);
  else if(ctrl_sel==1) t=__builtin_amdgcn_mov_dpp(__float_as_int(v),0x4E,0xF,0xF,true);
  else t=__builtin_amdgcn_mov_dpp(__float_as_int(v),0x141,0xF,0xF,true);
  return v+__int_as_float(t);
}
DI float lo16f(unsigned u){ return __uint_as_float(u<<16); }
DI float hi16f(unsigned u){ return __uint_as_float(u&0xffff0000u); }
DI float dot2bf(unsigned a, unsigned b, float c){
  return __builtin_amdgcn_fdot2_f32_bf16(__builtin_bit_cast(bf16x2,a),__builtin_bit_cast(bf16x2,b),c,false); }

#define TL_BEGIN()
#define DT(i)
#define P1(i)
#define TL_END(k)
constexpr int HP=1408;
constexpr int K1_WST=24576, K1_NST=4, K1_X=K1_WST*K1_NST, K1_LDS=K1_X+8*8192;

template<int NT> DI void gu_stage(lds_cptr xa, lds_cptr wa, int xo0, int xo1, f32x16 (&ag)[2], f32x16 (&au)[2]){
  #pragma unroll
  for(int s=0;s<2;s++){
    bf16x8 gf=cat8(lds_tr(wa+s*6144), lds_tr(wa+s*6144+1536));
    bf16x8 uf=cat8(lds_tr(wa+12288+s*6144), lds_tr(wa+12288+s*6144+1536));
    #pragma unroll
    for(int t=0;t<NT;t++){
      bf16x8 xf=lds_ld(xa+t*4096+(s?xo1:xo0));
      ag[t]=MFMA32(gf,xf,ag[t]); au[t]=MFMA32(uf,xf,au[t]);
    }
  }
}
DI void gu_store(const f32x16& ag, const f32x16& au, u16* hp, bool zero){
  u32x4 o0,o1;
  #pragma unroll
  for(int i=0;i<8;i++){
    float hv[2];
    #pragma unroll
    for(int e=0;e<2;e++){ float g=ag[2*i+e]; float sg=g*__builtin_amdgcn_rcpf(1.0f+__builtin_amdgcn_exp2f(-1.4426950408889634f*g)); hv[e]=zero?0.f:sg*au[2*i+e]; }
    unsigned w=pk2(hv[0],hv[1]); if(i<4) o0[i]=w; else o1[i-4]=w;
  }
  *(u32x4*)hp=o0; *(u32x4*)(hp+16)=o1;
}

__global__ __launch_bounds__(512,2) void k1_qkv(const u16* __restrict__ x, const u16* __restrict__ wqkv,
     u16* __restrict__ Qs, u16* __restrict__ Ks, u16* __restrict__ Vt,
     const u16* __restrict__ wg, const u16* __restrict__ wu, u16* __restrict__ hb, unsigned* __restrict__ ctl){
  extern __shared__ __attribute__((aligned(16))) char lds[];
  const int tid=threadIdx.x, lane=tid&63; const int wid=__builtin_amdgcn_readfirstlane(tid>>6);
  const int b=blockIdx.x;
  const int rb=(b&7)*4+((b>>3)&3), hg=b>>5;
  const unsigned lds0=(unsigned)(uintptr_t)lds;
  const int rr=lane>>3, p=lane&7;
  const int lrow=wid*8+rr;
  TL_BEGIN();
  P1(0);
  const u16* xp=x+(long)(rb*64+lrow)*D+p*8;
  u32x4 xv[8];
  #pragma unroll
  for(int i=0;i<8;i++) asm volatile("global_load_dwordx4 %0, %1, off offset:%2" : "=v"(xv[i]) : "v"(xp), "n"(i*128) : "memory");
  const u16* wp=wqkv+(long)lrow*(3*D)+hg*64+(((p>>1)^(((lrow>>1)&1)|(((lrow>>3)&1)<<1)))*16)+(p&1)*8;
  asm volatile("s_barrier":::"memory");
  #pragma unroll
  for(int st=0;st<K1_NST;st++){
    #pragma unroll
    for(int ty=0;ty<3;ty++) glds16(wp+(long)st*64*(3*D)+ty*D, lds0+st*K1_WST+ty*8192+wid*1024);
  }
  const int ft0=hg*5+(hg<3?hg:3);
  int goff[3];
  #pragma unroll
  for(int i=0;i<3;i++){
    const int pc=wid*3+i; const int i16=(pc%12)*64+lane; const int k=i16/24, ch=i16%24;
    int col=ft0*32+(((ch>>2)^((k>>1)&1))*32)+(ch&3)*8; col=col>F-8?F-8:col;
    goff[i]=k*F+col;
  }
  const int rot=GROT;
  const u16* gbase=(wid<4)?wg:wu;
#define GU_ISS(g_) do{ _Pragma("unroll") for(int i=0;i<3;i++) glds16(gbase+(long)(((g_)+rot)&15)*32*F+goff[i], lds0+((g_)&3)*K1_WST+(wid*3+i)*1024); }while(0)
  asm volatile("s_waitcnt vmcnt(12)" : "+v"(xv[0]),"+v"(xv[1]),"+v"(xv[2]),"+v"(xv[3]),"+v"(xv[4]),"+v"(xv[5]),"+v"(xv[6]),"+v"(xv[7]) :: "memory");
  P1(1);
  float s1=0.f, s2=0.f;
  #pragma unroll
  for(int i=0;i<8;i++){
    #pragma unroll
    for(int j=0;j<4;j++){ s1=dot2bf(xv[i][j],0x3F803F80u,s1); s2=dot2bf(xv[i][j],xv[i][j],s2); }
  }
  s1=dpp_add(s1,0); s2=dpp_add(s2,0); s1=dpp_add(s1,1); s2=dpp_add(s2,1); s1=dpp_add(s1,2); s2=dpp_add(s2,2);
  const float mean=s1*(1.0f/D);
  float var=s2*(1.0f/D)-mean*mean;
  if(__builtin_expect(__any(!(mean*mean<=16.f*var)),0)){
    float q2=0.f;
    #pragma unroll
    for(int i=0;i<8;i++){
      #pragma unroll
      for(int j=0;j<4;j++){ float a=lo16f(xv[i][j])-mean, c=hi16f(xv[i][j])-mean; q2=fmaf(a,a,q2); q2=fmaf(c,c,q2); }
    }
    q2=dpp_add(q2,0); q2=dpp_add(q2,1); q2=dpp_add(q2,2);
    var=q2*(1.0f/D);
  }
  const float mb=rbf(mean);
  const float den=rbf(sqrtf(rbf(rbf(var)+1e-5f)));
  const float rinv=1.0f/den;
  char* xw=lds+K1_X+lrow*128+((p^((lrow>>1)&7))*16);
#define NRM1(i,j) do{ unsigned c_=pk2(lo16f(xv[i][j])-mb, hi16f(xv[i][j])-mb); xv[i][j]=pk2(lo16f(c_)*rinv, hi16f(c_)*rinv); }while(0)
#define NORM(i) do{ NRM1(i,0); NRM1(i,1); NRM1(i,2); NRM1(i,3); *(u32x4*)(xw+(i)*8192)=xv[i]; }while(0)
  NORM(0); NORM(1);
  const int rt2=wid&1, hd=wid>>1;
  const int g=lane>>4, r16=lane&15;
  const int xsw=(r16>>1)&7;
  lds_cptr xa0=(lds_cptr)lds+K1_X+(rt2*32+r16)*128+((g^xsw)*16);
  lds_cptr xa1=(lds_cptr)lds+K1_X+(rt2*32+r16)*128+(((4+g)^xsw)*16);
  const int q4=(lane&15)>>2, p4=lane&3;
  const int fk=((q4>>1)&1)|((g&1)<<1);
  lds_cptr wa=(lds_cptr)lds+(8*g+q4)*128+((hd^fk)*32)+p4*8;
  f32x4 aq[2]={}, ak[2]={}, av[2]={};
  bf16x8 fx[2][2][2], fw[2][2][3];
#define TRW(set,s,ty,c) fw[set][s][ty]=cat8(lds_tr(wa+((c)%K1_NST)*K1_WST+(ty)*8192+(s)*4096), lds_tr(wa+((c)%K1_NST)*K1_WST+(ty)*8192+(s)*4096+512))
  asm volatile("s_waitcnt vmcnt(9) lgkmcnt(0)\n\ts_barrier":::"memory");
  P1(2);
  fx[0][0][0]=lds_ld(xa0); fx[0][0][1]=lds_ld(xa0+2048); fx[0][1][0]=lds_ld(xa1); fx[0][1][1]=lds_ld(xa1+2048);
  TRW(0,0,0,0); TRW(0,0,1,0); TRW(0,0,2,0); TRW(0,1,0,0); TRW(0,1,1,0); TRW(0,1,2,0);
#define K1_ISS1(c,i) do{ if((c)<4) glds16(wp+(long)((c)+4)*64*(3*D)+(i)*D, lds0+(c)*K1_WST+(i)*8192+wid*1024); \
                        else if((c)<7) glds16(gbase+(long)(((c)-4+rot)&15)*32*F+goff[i], lds0+((c)-4)*K1_WST+(wid*3+(i))*1024); }while(0)
  #pragma unroll
  for(int c=0;c<8;c++){
    const int cs=c&1, ns=cs^1; const int xb=(c+1)*8192; const int ni=(c+2)&7;
    if(c<7) asm volatile("s_waitcnt vmcnt(6) lgkmcnt(0)\n\ts_barrier":::"memory");
    if(c<7){ fx[ns][0][0]=lds_ld(xa0+xb); fx[ns][0][1]=lds_ld(xa0+xb+2048); TRW(ns,0,0,c+1); }
    aq[0]=MFMA16(fw[cs][0][0],fx[cs][0][0],aq[0]); aq[1]=MFMA16(fw[cs][0][0],fx[cs][0][1],aq[1]); ak[0]=MFMA16(fw[cs][0][1],fx[cs][0][0],ak[0]);
    if(c<6) NRM1(ni,0);
    K1_ISS1(c,0);
    SB();
    if(c<7){ TRW(ns,0,1,c+1); TRW(ns,0,2,c+1); }
    ak[1]=MFMA16(fw[cs][0][1],fx[cs][0][1],ak[1]); av[0]=MFMA16(fx[cs][0][0],fw[cs][0][2],av[0]); av[1]=MFMA16(fx[cs][0][1],fw[cs][0][2],av[1]);
    if(c<6) NRM1(ni,1);
    K1_ISS1(c,1);
    SB();
    if(c<7){ fx[ns][1][0]=lds_ld(xa1+xb); fx[ns][1][1]=lds_ld(xa1+xb+2048); TRW(ns,1,0,c+1); }
    aq[0]=MFMA16(fw[cs][1][0],fx[cs][1][0],aq[0]); aq[1]=MFMA16(fw[cs][1][0],fx[cs][1][1],aq[1]); ak[0]=MFMA16(fw[cs][1][1],fx[cs][1][0],ak[0]);
    if(c<6) NRM1(ni,2);
    K1_ISS1(c,2);
    SB();
    if(c<7){ TRW(ns,1,1,c+1); TRW(ns,1,2,c+1); }
    ak[1]=MFMA16(fw[cs][1][1],fx[cs][1][1],ak[1]); av[0]=MFMA16(fx[cs][1][0],fw[cs][1][2],av[0]); av[1]=MFMA16(fx[cs][1][1],fw[cs][1][2],av[1]);
    if(c<6){ NRM1(ni,3); *(u32x4*)(xw+ni*8192)=xv[ni]; }
    SB();
  }
  P1(3);
  {
    const int h=hg*4+hd; const int T=rb*2+rt2;
    #pragma unroll
    for(int tt=0;tt<2;tt++){
      const long qo=(long)h*S*DH+T*512+(g>>1)*256+(tt*16+r16)*8+(g&1)*4;
      u32x2 wq={pk2(aq[tt][0]*QSCALE,aq[tt][1]*QSCALE),pk2(aq[tt][2]*QSCALE,aq[tt][3]*QSCALE)};
      u32x2 wk={pk2(ak[tt][0],ak[tt][1]),pk2(ak[tt][2],ak[tt][3])};
      const long vo=(long)h*S*DH+(2*T+tt)*256+(g&1)*128+r16*8+(g>>1)*4;
      u32x2 wv={pk2(av[tt][0],av[tt][1]),pk2(av[tt][2],av[tt][3])};
      asm volatile("global_store_dwordx2 %0, %1, off\n\tglobal_store_dwordx2 %2, %3, off\n\tglobal_store_dwordx2 %4, %5, off"
         :: "v"(Qs+qo),"v"(wq),"v"(Ks+qo),"v"(wk),"v"(Vt+vo),"v"(wv) : "memory");
    }
  }
  {
    const int r=lane&31, hh=lane>>5;
    const int ftl = wid<4 ? wid : 4+((wid-4)>>1);
    const int tt0 = wid<4 ? 0 : ((wid-4)&1);
    const int xsw2=(r>>1)&7;
    lds_cptr xg=(lds_cptr)lds+K1_X+(tt0*32+r)*128;
    const int blk=(lane>>4)&1;
    lds_cptr wg_=(lds_cptr)lds+(8*hh+q4)*384+((ftl^((q4>>1)&1))*64)+blk*32+p4*8;
    f32x16 ag[2]={}, au[2]={};
    bf16x8 gf[2][2], uf[2][2], xf[2][2][2];
    const bool two=(wid<4);
#define GRD(gs_,s) do{ const int st_=(gs_)&1; lds_cptr w_=wg_+((gs_)&3)*K1_WST+(s)*6144; \
      gf[st_][s]=cat8(lds_tr(w_), lds_tr(w_+1536)); uf[st_][s]=cat8(lds_tr(w_+12288), lds_tr(w_+12288+1536)); \
      const int gr_=((gs_)+rot)&15; lds_cptr x_=xg+(gr_>>1)*8192+(((4*(gr_&1)+2*(s)+hh)^xsw2)*16); xf[st_][s][0]=lds_ld(x_); if(two) xf[st_][s][1]=lds_ld(x_+4096); }while(0)
#define GMM(gs_,s) do{ const int st_=(gs_)&1; ag[0]=MFMA32(gf[st_][s],xf[st_][s][0],ag[0]); au[0]=MFMA32(uf[st_][s],xf[st_][s][0],au[0]); \
      if(two){ ag[1]=MFMA32(gf[st_][s],xf[st_][s][1],ag[1]); au[1]=MFMA32(uf[st_][s],xf[st_][s][1],au[1]); } }while(0)
#define GU_ISS1(g_,i) glds16(gbase+(long)(((g_)+rot)&15)*32*F+goff[i], lds0+((g_)&3)*K1_WST+(wid*3+(i))*1024)
    asm volatile("s_waitcnt vmcnt(12) lgkmcnt(0)\n\ts_barrier":::"memory");
    P1(4);
    GRD(0,0); GRD(0,1);
    GU_ISS(3);
    #pragma unroll
    for(int gs=0;gs<16;gs++){
      if(gs==0) asm volatile("s_waitcnt vmcnt(12) lgkmcnt(0)\n\ts_barrier":::"memory");
      else if(gs==1) asm volatile("s_waitcnt vmcnt(12) lgkmcnt(0)\n\ts_barrier":::"memory");
      else if(gs<13) asm volatile("s_waitcnt vmcnt(6) lgkmcnt(0)\n\ts_barrier":::"memory");
      else if(gs==13) asm volatile("s_waitcnt vmcnt(3) lgkmcnt(0)\n\ts_barrier":::"memory");
      else if(gs==14) asm volatile("s_waitcnt vmcnt(0) lgkmcnt(0)\n\ts_barrier":::"memory");
      if(gs<15) GRD(gs+1,0);
      SB();
      GMM(gs,0);
      SB();
      if(gs<15) GRD(gs+1,1);
      SB();
      GMM(gs,1);
      if(gs+4<16){ GU_ISS1(gs+4,0); GU_ISS1(gs+4,1); GU_ISS1(gs+4,2); }
      SB();
    }
    P1(5);
    const int ft=ft0+ftl;
    const bool own=(ftl<5)||(hg<3)||(hg==7);
    if(own){
      u16* hp=hb+(long)(rb*64+tt0*32+r)*HP+ft*32+hh*8;
      gu_store(ag[0],au[0],hp,ft>=43);
      if(wid<4) gu_store(ag[1],au[1],hp+(long)32*HP,false);
    }
  }
  if(b==0) ctl[tid]=0u;
  TL_END(0);
}

#define GU_PRIO 2
constexpr int A2_K=0, A2_V=32768, A2_C=65536, K2_LDS=65536+4096;
#define EX(x) __builtin_amdgcn_exp2f(x)
DI void pv_step(const f32x16& sc, bf16x8 v0, bf16x8 v1, f32x16& o){
  u32x4 p0, p1;
  #pragma unroll
  for(int i=0;i<4;i++){ p0[i]=pk2(EX(sc[2*i]),EX(sc[2*i+1])); }
  #pragma unroll
  for(int i=0;i<4;i++){ p1[i]=pk2(EX(sc[8+2*i]),EX(sc[8+2*i+1])); }
  o=MFMA32(v0,__builtin_bit_cast(bf16x8,p0),o);
  o=MFMA32(v1,__builtin_bit_cast(bf16x8,p1),o);
}
DI void attn_role(char* lds, const u16* __restrict__ Qs, const u16* __restrict__ Ks, const u16* __restrict__ Vt, u16* O, int b, unsigned* ctl){
  const int tid=threadIdx.x, lane=tid&63; const int wid=__builtin_amdgcn_readfirstlane(tid>>6);
  const int h=(b&7)*4+((b>>3)>>3), qb=(b>>3)&7;
  const int r=lane&31, hh=lane>>5;
  const unsigned lds0=(unsigned)(uintptr_t)lds;
  const u16* qp=Qs+(long)h*S*DH+(qb*8+wid)*512+lane*8;
  bf16x8 qf;
  asm volatile("global_load_dwordx4 %0, %1, off" : "=v"(qf) : "v"(qp) : "memory");
  const u16* kg=Ks+(long)h*S*DH+wid*512+lane*8;
  const u16* vg=Vt+(long)h*S*DH+wid*512+lane*8;
#define A_ISSUE(c) do{ glds16(kg+(c)*4096, lds0+A2_K+((c)&3)*8192+wid*1024); glds16(vg+(c)*4096, lds0+A2_V+((c)&3)*8192+wid*1024); }while(0)
  A_ISSUE(0); A_ISSUE(1); A_ISSUE(2); A_ISSUE(3);
  { u32x2 v; v[0]=((tid&31)<2)?0x3F803F80u:0u; v[1]=v[0]; *(u32x2*)(lds+A2_C+tid*8)=v; }
  lds_cptr kp=(lds_cptr)lds+A2_K+lane*16;
  const bool cl=(r>=16);
  lds_cptr vp=(lds_cptr)lds+(cl ? A2_C : A2_V)+hh*256+(r&15)*16;
  const int vinc= cl?0:1024; const int vwrap= cl?0:(1024-32768);
  asm volatile("s_waitcnt vmcnt(6) lgkmcnt(0)\n\ts_barrier": "+v"(qf) ::"memory");
  f32x16 negm; f32x16 sA, sB;
  bf16x8 kA, kB, vA0, vA1, vB0, vB1;
  {
    f32x16 z={}; bf16x8 k0=lds_ld(kp);
    kA=lds_ld(kp+1024); vA0=lds_ld(vp); vA1=lds_ld(vp+512); vp+=vinc;
    f32x16 s0=MFMA32(k0,qf,z);
    float m=s0[0];
    #pragma unroll
    for(int i=1;i<16;i++) m=fmaxf(m,s0[i]);
    auto rr=__builtin_amdgcn_permlane32_swap(__float_as_uint(m),__float_as_uint(m),false,false);
    m=fmaxf(__uint_as_float(rr[0]),__uint_as_float(rr[1]));
    float nm=-(m+60.f);
    #pragma unroll
    for(int i=0;i<16;i++){ negm[i]=nm; sA[i]=s0[i]+nm; }
  }
  f32x16 o={};
#define STEP(sC,sN,kU,kL,vU0,vU1,vL0,vL1,t) do{ \
    u32x4 p0,p1; \
    kL=lds_ld(kp+(((t)+2)&31)*1024); vL0=lds_ld(vp); vL1=lds_ld(vp+512); vp+=((((t)+1)&31)==31)?vwrap:vinc; \
    float e0=EX(sC[0]),e1=EX(sC[1]),e2=EX(sC[2]),e3=EX(sC[3]); SB(); \
    sN=MFMA32(kU,qf,negm); SB(); \
    float e4=EX(sC[4]),e5=EX(sC[5]),e6=EX(sC[6]),e7=EX(sC[7]); \
    p0[0]=pk2(e0,e1); p0[1]=pk2(e2,e3); p0[2]=pk2(e4,e5); p0[3]=pk2(e6,e7); SB(); \
    float e8=EX(sC[8]),e9=EX(sC[9]); SB(); \
    o=MFMA32(vU0,__builtin_bit_cast(bf16x8,p0),o); SB(); \
    float e10=EX(sC[10]),e11=EX(sC[11]),e12=EX(sC[12]),e13=EX(sC[13]),e14=EX(sC[14]),e15=EX(sC[15]); \
    p1[0]=pk2(e8,e9); p1[1]=pk2(e10,e11); p1[2]=pk2(e12,e13); p1[3]=pk2(e14,e15); SB(); \
    o=MFMA32(vU1,__builtin_bit_cast(bf16x8,p1),o); SB(); \
  }while(0)
#define ITER(t) do{ STEP(sA,sB,kA,kB,vA0,vA1,vB0,vB1,(t)); STEP(sB,sA,kB,kA,vB0,vB1,vA0,vA1,(t)+1); }while(0)
  #pragma unroll
  for(int j=0;j<8;j++){
    ITER(8*j); ITER(8*j+2); ITER(8*j+4);
    if(j<7){
      if(j==0) asm volatile("s_waitcnt vmcnt(4) lgkmcnt(0)\n\ts_barrier":::"memory");
      else if(j<6) asm volatile("s_waitcnt vmcnt(2) lgkmcnt(0)\n\ts_barrier":::"memory");
      else asm volatile("s_waitcnt vmcnt(0) lgkmcnt(0)\n\ts_barrier":::"memory");
      if(j>=1 && j+3<8) A_ISSUE(j+3);
    }
    ITER(8*j+6);
  }
  float l=o[8];
  { auto rr=__builtin_amdgcn_permlane32_swap(__float_as_uint(l),__float_as_uint(l),false,false); l=__uint_as_float(rr[0]); }
  bool bad = !(l<1e30f);
  #pragma unroll
  for(int i=0;i<8;i++) bad = bad || !(fabsf(o[i])<1e37f);
  if(__builtin_expect(__any(bad),0)){
    const u16* kgl=Ks+(long)h*S*DH+lane*8;
    const u16* vgl=Vt+(long)h*S*DH+hh*128+(r&15)*8;
    float m=-INFINITY; f32x16 z={};
    #pragma unroll
    for(int i=0;i<16;i++) o[i]=0.f;
    for(int t=0;t<64;t++){
      bf16x8 kf=*(const bf16x8*)(kgl+t*512);
      bf16x8 v0=*(const bf16x8*)(vgl+t*512), v1=*(const bf16x8*)(vgl+t*512+256);
      if(cl){ u32x4 one={0x3F803F80u,0x3F803F80u,0x3F803F80u,0x3F803F80u}; v0=__builtin_bit_cast(bf16x8,one); v1=v0; }
      f32x16 s0=MFMA32(kf,qf,z);
      float tm=s0[0];
      #pragma unroll
      for(int i=1;i<16;i++) tm=fmaxf(tm,s0[i]);
      auto rr=__builtin_amdgcn_permlane32_swap(__float_as_uint(tm),__float_as_uint(tm),false,false);
      tm=fmaxf(__uint_as_float(rr[0]),__uint_as_float(rr[1]));
      float mn=fmaxf(m,tm);
      float alpha=EX(m-mn); m=mn;
      #pragma unroll
      for(int i=0;i<16;i++){ o[i]*=alpha; s0[i]-=mn; }
      pv_step(s0,v0,v1,o);
    }
    l=o[8];
    auto rr=__builtin_amdgcn_permlane32_swap(__float_as_uint(l),__float_as_uint(l),false,false); l=__uint_as_float(rr[0]);
  }
  float rl=1.0f/l;
  u32x4 w={pk2(o[0]*rl,o[1]*rl),pk2(o[2]*rl,o[3]*rl),pk2(o[4]*rl,o[5]*rl),pk2(o[6]*rl,o[7]*rl)};
  asm volatile("global_store_dwordx4 %0, %1, off sc1\n\ts_waitcnt vmcnt(0)\n\ts_barrier" :: "v"(O+(long)((qb*8+wid)*32+r)*D+h*DH+hh*8), "v"(w) : "memory");
  if(tid==0) __hip_atomic_fetch_add(ctl+qb*32,1u,__ATOMIC_RELAXED,__HIP_MEMORY_SCOPE_AGENT);
}
DI void down_role(char* lds, const u16* __restrict__ x, const u16* __restrict__ hb, const u16* __restrict__ wd, const u16* ob, const u16* __restrict__ wo, u16* __restrict__ out, unsigned* ctl, int mt, int nt){
  const int tid=threadIdx.x, lane=tid&63; const int wid=__builtin_amdgcn_readfirstlane(tid>>6);
  const unsigned lds0=(unsigned)(uintptr_t)lds;
  __builtin_amdgcn_s_setprio(GU_PRIO);
  for(int i=0;i<GU_DELAY;i++) __builtin_amdgcn_s_sleep(16);
  const int rr=lane>>3, sl=lane&7;
  const int prow=wid*8+rr;
  const u16* hs=hb+(long)(mt*64+prow)*HP+((sl^((prow>>1)&7))*8);
  const int wc=nt*64+(((sl>>2)^((prow>>1)&1))*32)+(sl&3)*8;
  auto issue=[&](int c){ const unsigned b_=lds0+(c&3)*16384+wid*1024; int k=c*64+prow; k=k>F-1?F-1:k;
    glds16(hs+c*64,b_); glds16(wd+(long)k*D+wc,b_+8192); };
  issue(0); issue(1); issue(2);
  const int wm=wid&1, wn=(wid>>1)&1, kh=wid>>2; const int r=lane&31, hh=lane>>5;
  const int xr=wm*32+r; const int xsw=(xr>>1)&7;
  lds_cptr xa0=(lds_cptr)lds+xr*128+(((4*kh+hh)^xsw)*16);
  lds_cptr xa1=(lds_cptr)lds+xr*128+(((4*kh+2+hh)^xsw)*16);
  const int blk=(lane>>4)&1, q4=(lane&15)>>2, p4=lane&3;
  lds_cptr wa=(lds_cptr)lds+8192+(32*kh+4*hh+q4)*128+((wn^((q4>>1)&1))*64)+blk*32+p4*8;
  f32x16 acc={};
  #pragma unroll 1
  for(int c=0;c<22;c++){
    if(c<20) asm volatile("s_waitcnt vmcnt(4) lgkmcnt(0)\n\ts_barrier":::"memory");
    else asm volatile("s_waitcnt vmcnt(0) lgkmcnt(0)\n\ts_barrier":::"memory");
    const int sb=(c&3)*16384;
    bf16x8 x0=lds_ld(xa0+sb), x1=lds_ld(xa1+sb);
    bf16x8 w0=cat8(lds_tr(wa+sb), lds_tr(wa+sb+1024));
    bf16x8 w1=cat8(lds_tr(wa+sb+2048), lds_tr(wa+sb+2048+1024));
    acc=MFMA32(w0,x0,acc); acc=MFMA32(w1,x1,acc);
    SB();
    if(c+3<22) issue(c+3);
  }
  DT(0);
  asm volatile("s_waitcnt lgkmcnt(0)\n\ts_barrier":::"memory");
  { const u16* ws_=wo+(long)prow*D+wc;
    #pragma unroll
    for(int c=0;c<8;c++) glds16(ws_+(long)c*64*D, lds0+c*8192+wid*1024); }
  const long ro=(long)(mt*64+xr)*D+nt*64+wn*32+4*hh;
  u32x2 xr4[4];
  #pragma unroll
  for(int q=0;q<4;q++) asm volatile("global_load_dwordx2 %0, %1, off offset:%2" : "=v"(xr4[q]) : "v"(x+ro), "n"(q*16) : "memory");
  asm volatile("s_waitcnt vmcnt(0)\n\ts_barrier" : "+v"(xr4[0]),"+v"(xr4[1]),"+v"(xr4[2]),"+v"(xr4[3]) :: "memory");
  bf16x8 wf[16];
  { lds_cptr wb=wa-8192;
    #pragma unroll
    for(int c=0;c<8;c++){
      wf[2*c]=cat8(lds_tr(wb+c*8192), lds_tr(wb+c*8192+1024));
      wf[2*c+1]=cat8(lds_tr(wb+c*8192+2048), lds_tr(wb+c*8192+2048+1024));
    } }
  asm volatile("s_waitcnt lgkmcnt(0)" : "+v"(wf[0]),"+v"(wf[1]),"+v"(wf[2]),"+v"(wf[3]),"+v"(wf[4]),"+v"(wf[5]),"+v"(wf[6]),"+v"(wf[7]),
      "+v"(wf[8]),"+v"(wf[9]),"+v"(wf[10]),"+v"(wf[11]),"+v"(wf[12]),"+v"(wf[13]),"+v"(wf[14]),"+v"(wf[15]) :: "memory");
  __builtin_amdgcn_s_setprio(0);
  if(tid==0){
    unsigned* cnt=ctl+(mt>>2)*32; unsigned spins=0;
    while(__hip_atomic_load(cnt,__ATOMIC_RELAXED,__HIP_MEMORY_SCOPE_AGENT)<32u){ __builtin_amdgcn_s_sleep(POLL_SLEEP); if(++spins>(1u<<24)) break; }
  }
  asm volatile("s_barrier":::"memory");
  DT(1);
  {
    const u16* os=ob+(long)(mt*64+prow)*D+sl*8;
    char* ow=lds+prow*128+((sl^((prow>>1)&7))*16);
    #pragma unroll
    for(int hb2=0;hb2<2;hb2++){
      u32x4 t0,t1,t2,t3;
      asm volatile("global_load_dwordx4 %0, %4, off offset:%5 sc1\n\tglobal_load_dwordx4 %1, %4, off offset:%6 sc1\n\tglobal_load_dwordx4 %2, %4, off offset:%7 sc1\n\tglobal_load_dwordx4 %3, %4, off offset:%8 sc1\n\ts_waitcnt vmcnt(0)"
        : "=&v"(t0),"=&v"(t1),"=&v"(t2),"=&v"(t3) : "v"(os), "n"(hb2*512), "n"(hb2*512+128), "n"(hb2*512+256), "n"(hb2*512+384) : "memory");
      *(u32x4*)(ow+(hb2*4+0)*8192)=t0; *(u32x4*)(ow+(hb2*4+1)*8192)=t1; *(u32x4*)(ow+(hb2*4+2)*8192)=t2; *(u32x4*)(ow+(hb2*4+3)*8192)=t3;
    } }
  asm volatile("s_waitcnt lgkmcnt(0)\n\ts_barrier":::"memory");
  DT(2);
  #pragma unroll
  for(int c=0;c<8;c++){
    bf16x8 x0=lds_ld(xa0+c*8192), x1=lds_ld(xa1+c*8192);
    acc=MFMA32(wf[2*c],x0,acc); acc=MFMA32(wf[2*c+1],x1,acc);
  }
  asm volatile("s_waitcnt lgkmcnt(0)\n\ts_barrier":::"memory");
  float* xch=(float*)lds+(wid&3)*1024+lane;
  if(kh==1){
    #pragma unroll
    for(int i=0;i<16;i++) xch[i*64]=acc[i];
  }
  __syncthreads();
  if(kh==1) return;
  #pragma unroll
  for(int q=0;q<4;q++){
    float v0=acc[4*q]+xch[(4*q)*64]+lo16f(xr4[q][0]), v1=acc[4*q+1]+xch[(4*q+1)*64]+hi16f(xr4[q][0]);
    float v2=acc[4*q+2]+xch[(4*q+2)*64]+lo16f(xr4[q][1]), v3=acc[4*q+3]+xch[(4*q+3)*64]+hi16f(xr4[q][1]);
    u32x2 w={pk2(v0,v1),pk2(v2,v3)};
    *(u32x2*)(out+ro+8*q)=w;
  }
}
__global__ __launch_bounds__(512,4) void k2_fused(const u16* __restrict__ Qs, const u16* __restrict__ Ks, const u16* __restrict__ Vt, u16* O,
     const u16* __restrict__ hb, const u16* __restrict__ x, const u16* __restrict__ wd, const u16* __restrict__ wo, u16* __restrict__ out, unsigned* ctl){
  extern __shared__ __attribute__((aligned(16))) char lds[];
  const int b=blockIdx.x;
  TL_BEGIN();
  if(b<256) attn_role(lds,Qs,Ks,Vt,O,b,ctl);
  else { const int u=b-256; down_role(lds,x,hb,wd,O,wo,out,ctl,(u&7)*4+((u>>3)&3),u>>5); }
  TL_END(1);
}

extern "C" void kernel_launch(void* const* d_in, const int* in_sizes, int n_in, void* d_out, int out_size, void* d_ws, size_t ws_size, hipStream_t stream){
  const u16* x=(const u16*)d_in[0]; const u16* wqkv=(const u16*)d_in[1]; const u16* wo=(const u16*)d_in[2];
  const u16* wg=(const u16*)d_in[4]; const u16* wu=(const u16*)d_in[5]; const u16* wd=(const u16*)d_in[6];
  char* ws=(char*)d_ws;
  unsigned* ctl=(unsigned*)ws; ws+=4096;
  u16* xn=(u16*)ws; ws+=S*D*2;
  u16* hb=(u16*)ws; ws+=(size_t)S*HP*2;
  u16* o=(u16*)ws; ws+=(size_t)S*D*2;
  u16* Qs=(u16*)ws; ws+=(size_t)S*D*2;
  u16* Ks=(u16*)ws; ws+=(size_t)S*D*2;
  u16* Vt=(u16*)ws; ws+=(size_t)S*D*2;
  float* yb=(float*)ws; ws+=(size_t)S*D*4;
  (void)hipFuncSetAttribute((const void*)k2_fused, hipFuncAttributeMaxDynamicSharedMemorySize, K2_LDS);
  (void)hipFuncSetAttribute((const void*)k1_qkv, hipFuncAttributeMaxDynamicSharedMemorySize, K1_LDS);
  k1_qkv<<<256,512,K1_LDS,stream>>>(x,wqkv,Qs,Ks,Vt,wg,wu,hb,ctl);
  k2_fused<<<512,512,K2_LDS,stream>>>(Qs,Ks,Vt,o,hb,x,wd,wo,(u16*)d_out,ctl);
}
```

```cpp
#define GROT ((((b>>3)&3)*4+((b&7)>>1))&15)
#define POLL_SLEEP 4
#define K1MAP 0
#define GU_DELAY 3
#define ABL 0
#define NBLK (256+352)
#define PACE 0
#define BARMODE 0
#define FIRSTW "0"
#include <hip/hip_runtime.h>
#include <hip/hip_bf16.h>
#include <stdint.h>

typedef unsigned short u16;
typedef __attribute__((ext_vector_type(8))) __bf16 bf16x8;
typedef __attribute__((ext_vector_type(2))) __bf16 bf16x2;
typedef __attribute__((ext_vector_type(2))) float f32x2;
typedef __attribute__((ext_vector_type(16))) float f32x16;
typedef __attribute__((ext_vector_type(4))) unsigned u32x4;
typedef __attribute__((ext_vector_type(2))) unsigned u32x2;
#define DI __device__ __forceinline__
#define MFMA32(a,b,c) __builtin_amdgcn_mfma_f32_32x32x16_bf16((a),(b),(c),0,0,0)

DI float bf2f(u16 b){ return __uint_as_float(((unsigned)b)<<16); }
DI u16 f2bf(float f){ unsigned u=__float_as_uint(f); u += 0x7fffu + ((u>>16)&1u); return (u16)(u>>16); }
DI float rbf(float f){ return bf2f(f2bf(f)); }
DI unsigned pk2(float lo, float hi){ f32x2 v={lo,hi}; bf16x2 b=__builtin_convertvector(v,bf16x2); return __builtin_bit_cast(unsigned,b); }

constexpr int S=2048, D=512, H=32, DH=16, F=1376;
constexpr float QSCALE = 0.25f*1.4426950408889634f;

DI void glds16(const void* g, unsigned lds_base) {
  unsigned sv; asm volatile("s_mov_b32 %0, m0\n\ts_mov_b32 m0, %2\n\ts_nop 0\n\tglobal_load_lds_dwordx4 %1, off\n\ts_mov_b32 m0, %0" : "=&s"(sv) : "v"(g), "s"(lds_base) : "memory"); }

typedef __attribute__((address_space(3))) const char* lds_cptr;
DI bf16x8 lds_ld(lds_cptr p){ return *(const __attribute__((address_space(3))) bf16x8*)p; }

#define SB() __builtin_amdgcn_sched_barrier(0)
typedef __attribute__((ext_vector_type(4))) float f32x4;
typedef __attribute__((ext_vector_type(4))) short s16x4;
typedef short v4i16_t __attribute__((ext_vector_type(4)));
#define MFMA16(a,b,c) __builtin_amdgcn_mfma_f32_16x16x32_bf16((a),(b),(c),0,0,0)
DI s16x4 lds_tr(lds_cptr p){ return __builtin_bit_cast(s16x4, __builtin_amdgcn_ds_read_tr16_b64_v4i16((__attribute__((address_space(3))) v4i16_t*)p)); }
DI bf16x8 cat8(s16x4 a, s16x4 b){ return __builtin_bit_cast(bf16x8, __builtin_shufflevector(a,b,0,1,2,3,4,5,6,7)); }
DI float dpp_add(float v, const int ctrl_sel){
  int t;
  if(ctrl_sel==0) t=__builtin_amdgcn_mov_dpp(__float_as_int(v),0xB1,0xF,0xF,true);
  else if(ctrl_sel==1) t=__builtin_amdgcn_mov_dpp(__float_as_int(v),0x4E,0xF,0xF,true);
  else t=__builtin_amdgcn_mov_dpp(__float_as_int(v),0x141,0xF,0xF,true);
  return v+__int_as_float(t);
}
DI float lo16f(unsigned u){ return __uint_as_float(u<<16); }
DI float hi16f(unsigned u){ return __uint_as_float(u&0xffff0000u); }
DI float dot2bf(unsigned a, unsigned b, float c){
  return __builtin_amdgcn_fdot2_f32_bf16(__builtin_bit_cast(bf16x2,a),__builtin_bit_cast(bf16x2,b),c,false); }

#define TL_BEGIN()
#define DT(i)
#define P1(i)
#define TL_END(k)
constexpr int HP=1408;
constexpr int K1_WST=24576, K1_NST=4, K1_X=K1_WST*K1_NST, K1_LDS=K1_X+8*8192;

template<int NT> DI void gu_stage(lds_cptr xa, lds_cptr wa, int xo0, int xo1, f32x16 (&ag)[2], f32x16 (&au)[2]){
  #pragma unroll
  for(int s=0;s<2;s++){
    bf16x8 gf=cat8(lds_tr(wa+s*6144), lds_tr(wa+s*6144+1536));
    bf16x8 uf=cat8(lds_tr(wa+12288+s*6144), lds_tr(wa+12288+s*6144+1536));
    #pragma unroll
    for(int t=0;t<NT;t++){
      bf16x8 xf=lds_ld(xa+t*4096+(s?xo1:xo0));
      ag[t]=MFMA32(gf,xf,ag[t]); au[t]=MFMA32(uf,xf,au[t]);
    }
  }
}
DI void gu_store(const f32x16& ag, const f32x16& au, u16* hp, bool zero){
  u32x4 o0,o1;
  #pragma unroll
  for(int i=0;i<8;i++){
    float hv[2];
    #pragma unroll
    for(int e=0;e<2;e++){ float g=ag[2*i+e]; float sg=g*__builtin_amdgcn_rcpf(1.0f+__builtin_amdgcn_exp2f(-1.4426950408889634f*g)); hv[e]=zero?0.f:sg*au[2*i+e]; }
    unsigned w=pk2(hv[0],hv[1]); if(i<4) o0[i]=w; else o1[i-4]=w;
  }
  *(u32x4*)hp=o0; *(u32x4*)(hp+16)=o1;
}

__global__ __launch_bounds__(512,2) void k1_qkv(const u16* __restrict__ x, const u16* __restrict__ wqkv,
     u16* __restrict__ Qs, u16* __restrict__ Ks, u16* __restrict__ Vt,
     const u16* __restrict__ wg, const u16* __restrict__ wu, u16* __restrict__ hb, unsigned* __restrict__ ctl){
  extern __shared__ __attribute__((aligned(16))) char lds[];
  const int tid=threadIdx.x, lane=tid&63; const int wid=__builtin_amdgcn_readfirstlane(tid>>6);
  const int b=blockIdx.x;
  const int rb=(b&7)*4+((b>>3)&3), hg=b>>5;
  const unsigned lds0=(unsigned)(uintptr_t)lds;
  const int rr=lane>>3, p=lane&7;
  const int lrow=wid*8+rr;
  TL_BEGIN();
  P1(0);
  const u16* xp=x+(long)(rb*64+lrow)*D+p*8;
  u32x4 xv[8];
  #pragma unroll
  for(int i=0;i<8;i++) asm volatile("global_load_dwordx4 %0, %1, off offset:%2" : "=v"(xv[i]) : "v"(xp), "n"(i*128) : "memory");
  const u16* wp=wqkv+(long)lrow*(3*D)+hg*64+(((p>>1)^(((lrow>>1)&1)|(((lrow>>3)&1)<<1)))*16)+(p&1)*8;
  asm volatile("s_barrier":::"memory");
  #pragma unroll
  for(int st=0;st<K1_NST;st++){
    #pragma unroll
    for(int ty=0;ty<3;ty++) glds16(wp+(long)st*64*(3*D)+ty*D, lds0+st*K1_WST+ty*8192+wid*1024);
  }
  const int ft0=hg*5+(hg<3?hg:3);
  int goff[3];
  #pragma unroll
  for(int i=0;i<3;i++){
    const int pc=wid*3+i; const int i16=(pc%12)*64+lane; const int k=i16/24, ch=i16%24;
    int col=ft0*32+(((ch>>2)^((k>>1)&1))*32)+(ch&3)*8; col=col>F-8?F-8:col;
    goff[i]=k*F+col;
  }
  const int rot=GROT;
  const u16* gbase=(wid<4)?wg:wu;
#define GU_ISS(g_) do{ _Pragma("unroll") for(int i=0;i<3;i++) glds16(gbase+(long)(((g_)+rot)&15)*32*F+goff[i], lds0+((g_)&3)*K1_WST+(wid*3+i)*1024); }while(0)
  asm volatile("s_waitcnt vmcnt(12)" : "+v"(xv[0]),"+v"(xv[1]),"+v"(xv[2]),"+v"(xv[3]),"+v"(xv[4]),"+v"(xv[5]),"+v"(xv[6]),"+v"(xv[7]) :: "memory");
  P1(1);
  float s1=0.f, s2=0.f;
  #pragma unroll
  for(int i=0;i<8;i++){
    #pragma unroll
    for(int j=0;j<4;j++){ s1=dot2bf(xv[i][j],0x3F803F80u,s1); s2=dot2bf(xv[i][j],xv[i][j],s2); }
  }
  s1=dpp_add(s1,0); s2=dpp_add(s2,0); s1=dpp_add(s1,1); s2=dpp_add(s2,1); s1=dpp_add(s1,2); s2=dpp_add(s2,2);
  const float mean=s1*(1.0f/D);
  float var=s2*(1.0f/D)-mean*mean;
  if(__builtin_expect(__any(!(mean*mean<=16.f*var)),0)){
    float q2=0.f;
    #pragma unroll
    for(int i=0;i<8;i++){
      #pragma unroll
      for(int j=0;j<4;j++){ float a=lo16f(xv[i][j])-mean, c=hi16f(xv[i][j])-mean; q2=fmaf(a,a,q2); q2=fmaf(c,c,q2); }
    }
    q2=dpp_add(q2,0); q2=dpp_add(q2,1); q2=dpp_add(q2,2);
    var=q2*(1.0f/D);
  }
  const float mb=rbf(mean);
  const float den=rbf(sqrtf(rbf(rbf(var)+1e-5f)));
  const float rinv=1.0f/den;
  char* xw=lds+K1_X+lrow*128+((p^((lrow>>1)&7))*16);
#define NRM1(i,j) do{ unsigned c_=pk2(lo16f(xv[i][j])-mb, hi16f(xv[i][j])-mb); xv[i][j]=pk2(lo16f(c_)*rinv, hi16f(c_)*rinv); }while(0)
#define NORM(i) do{ NRM1(i,0); NRM1(i,1); NRM1(i,2); NRM1(i,3); *(u32x4*)(xw+(i)*8192)=xv[i]; }while(0)
  NORM(0); NORM(1);
  const int rt2=wid&1, hd=wid>>1;
  const int g=lane>>4, r16=lane&15;
  const int xsw=(r16>>1)&7;
  lds_cptr xa0=(lds_cptr)lds+K1_X+(rt2*32+r16)*128+((g^xsw)*16);
  lds_cptr xa1=(lds_cptr)lds+K1_X+(rt2*32+r16)*128+(((4+g)^xsw)*16);
  const int q4=(lane&15)>>2, p4=lane&3;
  const int fk=((q4>>1)&1)|((g&1)<<1);
  lds_cptr wa=(lds_cptr)lds+(8*g+q4)*128+((hd^fk)*32)+p4*8;
  f32x4 aq[2]={}, ak[2]={}, av[2]={};
  bf16x8 fx[2][2][2], fw[2][2][3];
#define TRW(set,s,ty,c) fw[set][s][ty]=cat8(lds_tr(wa+((c)%K1_NST)*K1_WST+(ty)*8192+(s)*4096), lds_tr(wa+((c)%K1_NST)*K1_WST+(ty)*8192+(s)*4096+512))
  asm volatile("s_waitcnt vmcnt(9) lgkmcnt(0)\n\ts_barrier":::"memory");
  P1(2);
  fx[0][0][0]=lds_ld(xa0); fx[0][0][1]=lds_ld(xa0+2048); fx[0][1][0]=lds_ld(xa1); fx[0][1][1]=lds_ld(xa1+2048);
  TRW(0,0,0,0); TRW(0,0,1,0); TRW(0,0,2,0); TRW(0,1,0,0); TRW(0,1,1,0); TRW(0,1,2,0);
#define K1_ISS1(c,i) do{ if((c)<4) glds16(wp+(long)((c)+4)*64*(3*D)+(i)*D, lds0+(c)*K1_WST+(i)*8192+wid*1024); \
                        else if((c)<7) glds16(gbase+(long)(((c)-4+rot)&15)*32*F+goff[i], lds0+((c)-4)*K1_WST+(wid*3+(i))*1024); }while(0)
  #pragma unroll
  for(int c=0;c<8;c++){
    const int cs=c&1, ns=cs^1; const int xb=(c+1)*8192; const int ni=(c+2)&7;
    if(c<7) asm volatile("s_waitcnt vmcnt(6) lgkmcnt(0)\n\ts_barrier":::"memory");
    if(c<7){ fx[ns][0][0]=lds_ld(xa0+xb); fx[ns][0][1]=lds_ld(xa0+xb+2048); TRW(ns,0,0,c+1); }
    aq[0]=MFMA16(fw[cs][0][0],fx[cs][0][0],aq[0]); aq[1]=MFMA16(fw[cs][0][0],fx[cs][0][1],aq[1]); ak[0]=MFMA16(fw[cs][0][1],fx[cs][0][0],ak[0]);
    if(c<6) NRM1(ni,0);
    K1_ISS1(c,0);
    SB();
    if(c<7){ TRW(ns,0,1,c+1); TRW(ns,0,2,c+1); }
    ak[1]=MFMA16(fw[cs][0][1],fx[cs][0][1],ak[1]); av[0]=MFMA16(fx[cs][0][0],fw[cs][0][2],av[0]); av[1]=MFMA16(fx[cs][0][1],fw[cs][0][2],av[1]);
    if(c<6) NRM1(ni,1);
    K1_ISS1(c,1);
    SB();
    if(c<7){ fx[ns][1][0]=lds_ld(xa1+xb); fx[ns][1][1]=lds_ld(xa1+xb+2048); TRW(ns,1,0,c+1); }
    aq[0]=MFMA16(fw[cs][1][0],fx[cs][1][0],aq[0]); aq[1]=MFMA16(fw[cs][1][0],fx[cs][1][1],aq[1]); ak[0]=MFMA16(fw[cs][1][1],fx[cs][1][0],ak[0]);
    if(c<6) NRM1(ni,2);
    K1_ISS1(c,2);
    SB();
    if(c<7){ TRW(ns,1,1,c+1); TRW(ns,1,2,c+1); }
    ak[1]=MFMA16(fw[cs][1][1],fx[cs][1][1],ak[1]); av[0]=MFMA16(fx[cs][1][0],fw[cs][1][2],av[0]); av[1]=MFMA16(fx[cs][1][1],fw[cs][1][2],av[1]);
    if(c<6){ NRM1(ni,3); *(u32x4*)(xw+ni*8192)=xv[ni]; }
    SB();
  }
  P1(3);
  {
    const int h=hg*4+hd; const int T=rb*2+rt2;
    #pragma unroll
    for(int tt=0;tt<2;tt++){
      const long qo=(long)h*S*DH+T*512+(g>>1)*256+(tt*16+r16)*8+(g&1)*4;
      u32x2 wq={pk2(aq[tt][0]*QSCALE,aq[tt][1]*QSCALE),pk2(aq[tt][2]*QSCALE,aq[tt][3]*QSCALE)};
      u32x2 wk={pk2(ak[tt][0],ak[tt][1]),pk2(ak[tt][2],ak[tt][3])};
      const long vo=(long)h*S*DH+(2*T+tt)*256+(g&1)*128+r16*8+(g>>1)*4;
      u32x2 wv={pk2(av[tt][0],av[tt][1]),pk2(av[tt][2],av[tt][3])};
      asm volatile("global_store_dwordx2 %0, %1, off\n\tglobal_store_dwordx2 %2, %3, off\n\tglobal_store_dwordx2 %4, %5, off"
         :: "v"(Qs+qo),"v"(wq),"v"(Ks+qo),"v"(wk),"v"(Vt+vo),"v"(wv) : "memory");
    }
  }
  {
    const int r=lane&31, hh=lane>>5;
    const int ftl = wid<4 ? wid : 4+((wid-4)>>1);
    const int tt0 = wid<4 ? 0 : ((wid-4)&1);
    const int xsw2=(r>>1)&7;
    lds_cptr xg=(lds_cptr)lds+K1_X+(tt0*32+r)*128;
    const int blk=(lane>>4)&1;
    lds_cptr wg_=(lds_cptr)lds+(8*hh+q4)*384+((ftl^((q4>>1)&1))*64)+blk*32+p4*8;
    f32x16 ag[2]={}, au[2]={};
    bf16x8 gf[2][2], uf[2][2], xf[2][2][2];
    const bool two=(wid<4);
#define GRD(gs_,s) do{ const int st_=(gs_)&1; lds_cptr w_=wg_+((gs_)&3)*K1_WST+(s)*6144; \
      gf[st_][s]=cat8(lds_tr(w_), lds_tr(w_+1536)); uf[st_][s]=cat8(lds_tr(w_+12288), lds_tr(w_+12288+1536)); \
      const int gr_=((gs_)+rot)&15; lds_cptr x_=xg+(gr_>>1)*8192+(((4*(gr_&1)+2*(s)+hh)^xsw2)*16); xf[st_][s][0]=lds_ld(x_); if(two) xf[st_][s][1]=lds_ld(x_+4096); }while(0)
#define GMM(gs_,s) do{ const int st_=(gs_)&1; ag[0]=MFMA32(gf[st_][s],xf[st_][s][0],ag[0]); au[0]=MFMA32(uf[st_][s],xf[st_][s][0],au[0]); \
      if(two){ ag[1]=MFMA32(gf[st_][s],xf[st_][s][1],ag[1]); au[1]=MFMA32(uf[st_][s],xf[st_][s][1],au[1]); } }while(0)
#define GU_ISS1(g_,i) glds16(gbase+(long)(((g_)+rot)&15)*32*F+goff[i], lds0+((g_)&3)*K1_WST+(wid*3+(i))*1024)
    asm volatile("s_waitcnt vmcnt(12) lgkmcnt(0)\n\ts_barrier":::"memory");
    P1(4);
    GRD(0,0); GRD(0,1);
    GU_ISS(3);
#define GWAIT(gs) do{ \
      if((gs)<2) asm volatile("s_waitcnt vmcnt(12) lgkmcnt(0)\n\ts_barrier":::"memory"); \
      else if((gs)<13) asm volatile("s_waitcnt vmcnt(6) lgkmcnt(0)\n\ts_barrier":::"memory"); \
      else if((gs)==13) asm volatile("s_waitcnt vmcnt(3) lgkmcnt(0)\n\ts_barrier":::"memory"); \
      else if((gs)==14) asm volatile("s_waitcnt vmcnt(0) lgkmcnt(0)\n\ts_barrier":::"memory"); }while(0)
#define RG(gn,s) do{ if((gn)<16){ lds_cptr w_=wg_+((gn)&3)*K1_WST+(s)*6144; gf[(gn)&1][s]=cat8(lds_tr(w_), lds_tr(w_+1536)); } }while(0)
#define RU(gn,s) do{ if((gn)<16){ lds_cptr w_=wg_+((gn)&3)*K1_WST+(s)*6144+12288; uf[(gn)&1][s]=cat8(lds_tr(w_), lds_tr(w_+1536)); } }while(0)
#define RX(gn,s,t) do{ if((gn)<16){ const int gr_=((gn)+rot)&15; xf[(gn)&1][s][t]=lds_ld(xg+(gr_>>1)*8192+(((4*(gr_&1)+2*(s)+hh)^xsw2)*16)+(t)*4096); } }while(0)
#define MG(gs,s,t) ag[t]=MFMA32(gf[(gs)&1][s],xf[(gs)&1][s][t],ag[t])
#define MU(gs,s,t) au[t]=MFMA32(uf[(gs)&1][s],xf[(gs)&1][s][t],au[t])
#define GI(gs,i) do{ if((gs)+4<16) GU_ISS1((gs)+4,i); }while(0)
    if(two){
      #pragma unroll
      for(int gs=0;gs<16;gs++){
        GWAIT(gs);
        MG(gs,0,0); RG(gs+1,0); SB();
        MU(gs,0,0); RU(gs+1,0); SB();
        MG(gs,0,1); RX(gs+1,0,0); RX(gs+1,0,1); SB();
        MU(gs,0,1); RG(gs+1,1); SB();
        MG(gs,1,0); RU(gs+1,1); SB();
        MU(gs,1,0); RX(gs+1,1,0); RX(gs+1,1,1); SB();
        MG(gs,1,1); GI(gs,0); GI(gs,1); SB();
        MU(gs,1,1); GI(gs,2); SB();
      }
    } else {
      #pragma unroll
      for(int gs=0;gs<16;gs++){
        GWAIT(gs);
        GI(gs,0); SB();
        MG(gs,0,0); RG(gs+1,0); RU(gs+1,0); SB();
        MU(gs,0,0); RX(gs+1,0,0); RG(gs+1,1); SB();
        MG(gs,1,0); RU(gs+1,1); RX(gs+1,1,0); SB();
        MU(gs,1,0); GI(gs,1); GI(gs,2); SB();
      }
    }
    P1(5);
    const int ft=ft0+ftl;
    const bool own=(ftl<5)||(hg<3)||(hg==7);
    if(own){
      u16* hp=hb+(long)(rb*64+tt0*32+r)*HP+ft*32+hh*8;
      gu_store(ag[0],au[0],hp,ft>=43);
      if(wid<4) gu_store(ag[1],au[1],hp+(long)32*HP,false);
    }
  }
  if(b==0) ctl[tid]=0u;
  TL_END(0);
}

#define GU_PRIO 2
constexpr int A2_K=0, A2_V=32768, A2_C=65536, K2_LDS=65536+4096;
#define EX(x) __builtin_amdgcn_exp2f(x)
DI void pv_step(const f32x16& sc, bf16x8 v0, bf16x8 v1, f32x16& o){
  u32x4 p0, p1;
  #pragma unroll
  for(int i=0;i<4;i++){ p0[i]=pk2(EX(sc[2*i]),EX(sc[2*i+1])); }
  #pragma unroll
  for(int i=0;i<4;i++){ p1[i]=pk2(EX(sc[8+2*i]),EX(sc[8+2*i+1])); }
  o=MFMA32(v0,__builtin_bit_cast(bf16x8,p0),o);
  o=MFMA32(v1,__builtin_bit_cast(bf16x8,p1),o);
}
DI void attn_role(char* lds, const u16* __restrict__ Qs, const u16* __restrict__ Ks, const u16* __restrict__ Vt, u16* O, int b, unsigned* ctl){
  const int tid=threadIdx.x, lane=tid&63; const int wid=__builtin_amdgcn_readfirstlane(tid>>6);
  const int h=(b&7)*4+((b>>3)>>3), qb=(b>>3)&7;
  const int r=lane&31, hh=lane>>5;
  const unsigned lds0=(unsigned)(uintptr_t)lds;
  const u16* qp=Qs+(long)h*S*DH+(qb*8+wid)*512+lane*8;
  bf16x8 qf;
  asm volatile("global_load_dwordx4 %0, %1, off" : "=v"(qf) : "v"(qp) : "memory");
  const u16* kg=Ks+(long)h*S*DH+wid*512+lane*8;
  const u16* vg=Vt+(long)h*S*DH+wid*512+lane*8;
#define A_ISSUE(c) do{ glds16(kg+(c)*4096, lds0+A2_K+((c)&3)*8192+wid*1024); glds16(vg+(c)*4096, lds0+A2_V+((c)&3)*8192+wid*1024); }while(0)
  A_ISSUE(0); A_ISSUE(1); A_ISSUE(2); A_ISSUE(3);
  { u32x2 v; v[0]=((tid&31)<2)?0x3F803F80u:0u; v[1]=v[0]; *(u32x2*)(lds+A2_C+tid*8)=v; }
  lds_cptr kp=(lds_cptr)lds+A2_K+lane*16;
  const bool cl=(r>=16);
  lds_cptr vp=(lds_cptr)lds+(cl ? A2_C : A2_V)+hh*256+(r&15)*16;
  const int vinc= cl?0:1024; const int vwrap= cl?0:(1024-32768);
  asm volatile("s_waitcnt vmcnt(6) lgkmcnt(0)\n\ts_barrier": "+v"(qf) ::"memory");
  f32x16 negm; f32x16 sA, sB;
  bf16x8 kA, kB, vA0, vA1, vB0, vB1;
  {
    f32x16 z={}; bf16x8 k0=lds_ld(kp);
    kA=lds_ld(kp+1024); vA0=lds_ld(vp); vA1=lds_ld(vp+512); vp+=vinc;
    f32x16 s0=MFMA32(k0,qf,z);
    float m=s0[0];
    #pragma unroll
    for(int i=1;i<16;i++) m=fmaxf(m,s0[i]);
    auto rr=__builtin_amdgcn_permlane32_swap(__float_as_uint(m),__float_as_uint(m),false,false);
    m=fmaxf(__uint_as_float(rr[0]),__uint_as_float(rr[1]));
    float nm=-(m+60.f);
    #pragma unroll
    for(int i=0;i<16;i++){ negm[i]=nm; sA[i]=s0[i]+nm; }
  }
  f32x16 o={};
#define STEP(sC,sN,kU,kL,vU0,vU1,vL0,vL1,t) do{ \
    u32x4 p0,p1; \
    kL=lds_ld(kp+(((t)+2)&31)*1024); vL0=lds_ld(vp); vL1=lds_ld(vp+512); vp+=((((t)+1)&31)==31)?vwrap:vinc; \
    float e0=EX(sC[0]),e1=EX(sC[1]),e2=EX(sC[2]),e3=EX(sC[3]); SB(); \
    sN=MFMA32(kU,qf,negm); SB(); \
    float e4=EX(sC[4]),e5=EX(sC[5]),e6=EX(sC[6]),e7=EX(sC[7]); \
    p0[0]=pk2(e0,e1); p0[1]=pk2(e2,e3); p0[2]=pk2(e4,e5); p0[3]=pk2(e6,e7); SB(); \
    float e8=EX(sC[8]),e9=EX(sC[9]); SB(); \
    o=MFMA32(vU0,__builtin_bit_cast(bf16x8,p0),o); SB(); \
    float e10=EX(sC[10]),e11=EX(sC[11]),e12=EX(sC[12]),e13=EX(sC[13]),e14=EX(sC[14]),e15=EX(sC[15]); \
    p1[0]=pk2(e8,e9); p1[1]=pk2(e10,e11); p1[2]=pk2(e12,e13); p1[3]=pk2(e14,e15); SB(); \
    o=MFMA32(vU1,__builtin_bit_cast(bf16x8,p1),o); SB(); \
  }while(0)
#define ITER(t) do{ STEP(sA,sB,kA,kB,vA0,vA1,vB0,vB1,(t)); STEP(sB,sA,kB,kA,vB0,vB1,vA0,vA1,(t)+1); }while(0)
  #pragma unroll
  for(int j=0;j<8;j++){
    ITER(8*j); ITER(8*j+2); ITER(8*j+4);
    if(j<7){
      if(j==0) asm volatile("s_waitcnt vmcnt(4) lgkmcnt(0)\n\ts_barrier":::"memory");
      else if(j<6) asm volatile("s_waitcnt vmcnt(2) lgkmcnt(0)\n\ts_barrier":::"memory");
      else asm volatile("s_waitcnt vmcnt(0) lgkmcnt(0)\n\ts_barrier":::"memory");
      if(j>=1 && j+3<8) A_ISSUE(j+3);
    }
    ITER(8*j+6);
  }
  float l=o[8];
  { auto rr=__builtin_amdgcn_permlane32_swap(__float_as_uint(l),__float_as_uint(l),false,false); l=__uint_as_float(rr[0]); }
  bool bad = !(l<1e30f);
  #pragma unroll
  for(int i=0;i<8;i++) bad = bad || !(fabsf(o[i])<1e37f);
  if(__builtin_expect(__any(bad),0)){
    const u16* kgl=Ks+(long)h*S*DH+lane*8;
    const u16* vgl=Vt+(long)h*S*DH+hh*128+(r&15)*8;
    float m=-INFINITY; f32x16 z={};
    #pragma unroll
    for(int i=0;i<16;i++) o[i]=0.f;
    for(int t=0;t<64;t++){
      bf16x8 kf=*(const bf16x8*)(kgl+t*512);
      bf16x8 v0=*(const bf16x8*)(vgl+t*512), v1=*(const bf16x8*)(vgl+t*512+256);
      if(cl){ u32x4 one={0x3F803F80u,0x3F803F80u,0x3F803F80u,0x3F803F80u}; v0=__builtin_bit_cast(bf16x8,one); v1=v0; }
      f32x16 s0=MFMA32(kf,qf,z);
      float tm=s0[0];
      #pragma unroll
      for(int i=1;i<16;i++) tm=fmaxf(tm,s0[i]);
      auto rr=__builtin_amdgcn_permlane32_swap(__float_as_uint(tm),__float_as_uint(tm),false,false);
      tm=fmaxf(__uint_as_float(rr[0]),__uint_as_float(rr[1]));
      float mn=fmaxf(m,tm);
      float alpha=EX(m-mn); m=mn;
      #pragma unroll
      for(int i=0;i<16;i++){ o[i]*=alpha; s0[i]-=mn; }
      pv_step(s0,v0,v1,o);
    }
    l=o[8];
    auto rr=__builtin_amdgcn_permlane32_swap(__float_as_uint(l),__float_as_uint(l),false,false); l=__uint_as_float(rr[0]);
  }
  float rl=1.0f/l;
  u32x4 w={pk2(o[0]*rl,o[1]*rl),pk2(o[2]*rl,o[3]*rl),pk2(o[4]*rl,o[5]*rl),pk2(o[6]*rl,o[7]*rl)};
  asm volatile("global_store_dwordx4 %0, %1, off sc1\n\ts_waitcnt vmcnt(0)\n\ts_barrier" :: "v"(O+(long)((qb*8+wid)*32+r)*D+h*DH+hh*8), "v"(w) : "memory");
  if(tid==0) __hip_atomic_fetch_add(ctl+qb*32,1u,__ATOMIC_RELAXED,__HIP_MEMORY_SCOPE_AGENT);
}
DI void down_role(char* lds, const u16* __restrict__ x, const u16* __restrict__ hb, const u16* __restrict__ wd, const u16* ob, const u16* __restrict__ wo, u16* __restrict__ out, unsigned* ctl, int mt, int nt){
  const int tid=threadIdx.x, lane=tid&63; const int wid=__builtin_amdgcn_readfirstlane(tid>>6);
  const unsigned lds0=(unsigned)(uintptr_t)lds;
  __builtin_amdgcn_s_setprio(GU_PRIO);
  for(int i=0;i<GU_DELAY;i++) __builtin_amdgcn_s_sleep(16);
  const int rr=lane>>3, sl=lane&7;
  const int prow=wid*8+rr;
  const u16* hs=hb+(long)(mt*64+prow)*HP+((sl^((prow>>1)&7))*8);
  const int wc=nt*64+(((sl>>2)^((prow>>1)&1))*32)+(sl&3)*8;
  auto issue=[&](int c){ const unsigned b_=lds0+(c&3)*16384+wid*1024; int k=c*64+prow; k=k>F-1?F-1:k;
    glds16(hs+c*64,b_); glds16(wd+(long)k*D+wc,b_+8192); };
  issue(0); issue(1); issue(2);
  const int wm=wid&1, wn=(wid>>1)&1, kh=wid>>2; const int r=lane&31, hh=lane>>5;
  const int xr=wm*32+r; const int xsw=(xr>>1)&7;
  lds_cptr xa0=(lds_cptr)lds+xr*128+(((4*kh+hh)^xsw)*16);
  lds_cptr xa1=(lds_cptr)lds+xr*128+(((4*kh+2+hh)^xsw)*16);
  const int blk=(lane>>4)&1, q4=(lane&15)>>2, p4=lane&3;
  lds_cptr wa=(lds_cptr)lds+8192+(32*kh+4*hh+q4)*128+((wn^((q4>>1)&1))*64)+blk*32+p4*8;
  f32x16 acc={};
  #pragma unroll 1
  for(int c=0;c<22;c++){
    if(c<20) asm volatile("s_waitcnt vmcnt(4) lgkmcnt(0)\n\ts_barrier":::"memory");
    else asm volatile("s_waitcnt vmcnt(0) lgkmcnt(0)\n\ts_barrier":::"memory");
    const int sb=(c&3)*16384;
    bf16x8 x0=lds_ld(xa0+sb), x1=lds_ld(xa1+sb);
    bf16x8 w0=cat8(lds_tr(wa+sb), lds_tr(wa+sb+1024));
    bf16x8 w1=cat8(lds_tr(wa+sb+2048), lds_tr(wa+sb+2048+1024));
    acc=MFMA32(w0,x0,acc); acc=MFMA32(w1,x1,acc);
    SB();
    if(c+3<22) issue(c+3);
  }
  DT(0);
  asm volatile("s_waitcnt lgkmcnt(0)\n\ts_barrier":::"memory");
  { const u16* ws_=wo+(long)prow*D+wc;
    #pragma unroll
    for(int c=0;c<8;c++) glds16(ws_+(long)c*64*D, lds0+c*8192+wid*1024); }
  const long ro=(long)(mt*64+xr)*D+nt*64+wn*32+4*hh;
  u32x2 xr4[4];
  #pragma unroll
  for(int q=0;q<4;q++) asm volatile("global_load_dwordx2 %0, %1, off offset:%2" : "=v"(xr4[q]) : "v"(x+ro), "n"(q*16) : "memory");
  asm volatile("s_waitcnt vmcnt(0)\n\ts_barrier" : "+v"(xr4[0]),"+v"(xr4[1]),"+v"(xr4[2]),"+v"(xr4[3]) :: "memory");
  bf16x8 wf[16];
  { lds_cptr wb=wa-8192;
    #pragma unroll
    for(int c=0;c<8;c++){
      wf[2*c]=cat8(lds_tr(wb+c*8192), lds_tr(wb+c*8192+1024));
      wf[2*c+1]=cat8(lds_tr(wb+c*8192+2048), lds_tr(wb+c*8192+2048+1024));
    } }
  asm volatile("s_waitcnt lgkmcnt(0)" : "+v"(wf[0]),"+v"(wf[1]),"+v"(wf[2]),"+v"(wf[3]),"+v"(wf[4]),"+v"(wf[5]),"+v"(wf[6]),"+v"(wf[7]),
      "+v"(wf[8]),"+v"(wf[9]),"+v"(wf[10]),"+v"(wf[11]),"+v"(wf[12]),"+v"(wf[13]),"+v"(wf[14]),"+v"(wf[15]) :: "memory");
  __builtin_amdgcn_s_setprio(0);
  if(tid==0){
    unsigned* cnt=ctl+(mt>>2)*32; unsigned spins=0;
    while(__hip_atomic_load(cnt,__ATOMIC_RELAXED,__HIP_MEMORY_SCOPE_AGENT)<32u){ __builtin_amdgcn_s_sleep(POLL_SLEEP); if(++spins>(1u<<24)) break; }
  }
  asm volatile("s_barrier":::"memory");
  DT(1);
  {
    const u16* os=ob+(long)(mt*64+prow)*D+sl*8;
    char* ow=lds+prow*128+((sl^((prow>>1)&7))*16);
    #pragma unroll
    for(int hb2=0;hb2<2;hb2++){
      u32x4 t0,t1,t2,t3;
      asm volatile("global_load_dwordx4 %0, %4, off offset:%5 sc1\n\tglobal_load_dwordx4 %1, %4, off offset:%6 sc1\n\tglobal_load_dwordx4 %2, %4, off offset:%7 sc1\n\tglobal_load_dwordx4 %3, %4, off offset:%8 sc1\n\ts_waitcnt vmcnt(0)"
        : "=&v"(t0),"=&v"(t1),"=&v"(t2),"=&v"(t3) : "v"(os), "n"(hb2*512), "n"(hb2*512+128), "n"(hb2*512+256), "n"(hb2*512+384) : "memory");
      *(u32x4*)(ow+(hb2*4+0)*8192)=t0; *(u32x4*)(ow+(hb2*4+1)*8192)=t1; *(u32x4*)(ow+(hb2*4+2)*8192)=t2; *(u32x4*)(ow+(hb2*4+3)*8192)=t3;
    } }
  asm volatile("s_waitcnt lgkmcnt(0)\n\ts_barrier":::"memory");
  DT(2);
  #pragma unroll
  for(int c=0;c<8;c++){
    bf16x8 x0=lds_ld(xa0+c*8192), x1=lds_ld(xa1+c*8192);
    acc=MFMA32(wf[2*c],x0,acc); acc=MFMA32(wf[2*c+1],x1,acc);
  }
  asm volatile("s_waitcnt lgkmcnt(0)\n\ts_barrier":::"memory");
  float* xch=(float*)lds+(wid&3)*1024+lane;
  if(kh==1){
    #pragma unroll
    for(int i=0;i<16;i++) xch[i*64]=acc[i];
  }
  __syncthreads();
  if(kh==1) return;
  #pragma unroll
  for(int q=0;q<4;q++){
    float v0=acc[4*q]+xch[(4*q)*64]+lo16f(xr4[q][0]), v1=acc[4*q+1]+xch[(4*q+1)*64]+hi16f(xr4[q][0]);
    float v2=acc[4*q+2]+xch[(4*q+2)*64]+lo16f(xr4[q][1]), v3=acc[4*q+3]+xch[(4*q+3)*64]+hi16f(xr4[q][1]);
    u32x2 w={pk2(v0,v1),pk2(v2,v3)};
    *(u32x2*)(out+ro+8*q)=w;
  }
}
__global__ __launch_bounds__(512,4) void k2_fused(const u16* __restrict__ Qs, const u16* __restrict__ Ks, const u16* __restrict__ Vt, u16* O,
     const u16* __restrict__ hb, const u16* __restrict__ x, const u16* __restrict__ wd, const u16* __restrict__ wo, u16* __restrict__ out, unsigned* ctl){
  extern __shared__ __attribute__((aligned(16))) char lds[];
  const int b=blockIdx.x;
  TL_BEGIN();
  if(b<256) attn_role(lds,Qs,Ks,Vt,O,b,ctl);
  else { const int u=b-256; down_role(lds,x,hb,wd,O,wo,out,ctl,(u&7)*4+((u>>3)&3),u>>5); }
  TL_END(1);
}

extern "C" void kernel_launch(void* const* d_in, const int* in_sizes, int n_in, void* d_out, int out_size, void* d_ws, size_t ws_size, hipStream_t stream){
  const u16* x=(const u16*)d_in[0]; const u16* wqkv=(const u16*)d_in[1]; const u16* wo=(const u16*)d_in[2];
  const u16* wg=(const u16*)d_in[4]; const u16* wu=(const u16*)d_in[5]; const u16* wd=(const u16*)d_in[6];
  char* ws=(char*)d_ws;
  unsigned* ctl=(unsigned*)ws; ws+=4096;
  u16* xn=(u16*)ws; ws+=S*D*2;
  u16* hb=(u16*)ws; ws+=(size_t)S*HP*2;
  u16* o=(u16*)ws; ws+=(size_t)S*D*2;
  u16* Qs=(u16*)ws; ws+=(size_t)S*D*2;
  u16* Ks=(u16*)ws; ws+=(size_t)S*D*2;
  u16* Vt=(u16*)ws; ws+=(size_t)S*D*2;
  float* yb=(float*)ws; ws+=(size_t)S*D*4;
  (void)hipFuncSetAttribute((const void*)k2_fused, hipFuncAttributeMaxDynamicSharedMemorySize, K2_LDS);
  (void)hipFuncSetAttribute((const void*)k1_qkv, hipFuncAttributeMaxDynamicSharedMemorySize, K1_LDS);
  k1_qkv<<<256,512,K1_LDS,stream>>>(x,wqkv,Qs,Ks,Vt,wg,wu,hb,ctl);
  k2_fused<<<512,512,K2_LDS,stream>>>(Qs,Ks,Vt,o,hb,x,wd,wo,(u16*)d_out,ctl);
}
```

```cpp
#define TGSEL ((__builtin_amdgcn_s_getreg((3<<11)|(16<<6)|4))&1u)
#define GROT ((((b>>3)&3)*4+((b&7)>>1))&15)
#define POLL_SLEEP 4
#define K1MAP 0
#define GU_DELAY 3
#define ABL 0
#define NBLK (256+352)
#define PACE 0
#define BARMODE 0
#define FIRSTW "0"
#include <hip/hip_runtime.h>
#include <hip/hip_bf16.h>
#include <stdint.h>

typedef unsigned short u16;
typedef __attribute__((ext_vector_type(8))) __bf16 bf16x8;
typedef __attribute__((ext_vector_type(2))) __bf16 bf16x2;
typedef __attribute__((ext_vector_type(2))) float f32x2;
typedef __attribute__((ext_vector_type(16))) float f32x16;
typedef __attribute__((ext_vector_type(4))) unsigned u32x4;
typedef __attribute__((ext_vector_type(2))) unsigned u32x2;
#define DI __device__ __forceinline__
#define MFMA32(a,b,c) __builtin_amdgcn_mfma_f32_32x32x16_bf16((a),(b),(c),0,0,0)

DI float bf2f(u16 b){ return __uint_as_float(((unsigned)b)<<16); }
DI u16 f2bf(float f){ unsigned u=__float_as_uint(f); u += 0x7fffu + ((u>>16)&1u); return (u16)(u>>16); }
DI float rbf(float f){ return bf2f(f2bf(f)); }
DI unsigned pk2(float lo, float hi){ f32x2 v={lo,hi}; bf16x2 b=__builtin_convertvector(v,bf16x2); return __builtin_bit_cast(unsigned,b); }

constexpr int S=2048, D=512, H=32, DH=16, F=1376;
constexpr float QSCALE = 0.25f*1.4426950408889634f;

DI void glds16(const void* g, unsigned lds_base) {
  unsigned sv; asm volatile("s_mov_b32 %0, m0\n\ts_mov_b32 m0, %2\n\ts_nop 0\n\tglobal_load_lds_dwordx4 %1, off\n\ts_mov_b32 m0, %0" : "=&s"(sv) : "v"(g), "s"(lds_base) : "memory"); }

typedef __attribute__((address_space(3))) const char* lds_cptr;
DI bf16x8 lds_ld(lds_cptr p){ return *(const __attribute__((address_space(3))) bf16x8*)p; }

#define SB() __builtin_amdgcn_sched_barrier(0)
typedef __attribute__((ext_vector_type(4))) float f32x4;
typedef __attribute__((ext_vector_type(4))) short s16x4;
typedef short v4i16_t __attribute__((ext_vector_type(4)));
#define MFMA16(a,b,c) __builtin_amdgcn_mfma_f32_16x16x32_bf16((a),(b),(c),0,0,0)
DI s16x4 lds_tr(lds_cptr p){ return __builtin_bit_cast(s16x4, __builtin_amdgcn_ds_read_tr16_b64_v4i16((__attribute__((address_space(3))) v4i16_t*)p)); }
DI bf16x8 cat8(s16x4 a, s16x4 b){ return __builtin_bit_cast(bf16x8, __builtin_shufflevector(a,b,0,1,2,3,4,5,6,7)); }
DI float dpp_add(float v, const int ctrl_sel){
  int t;
  if(ctrl_sel==0) t=__builtin_amdgcn_mov_dpp(__float_as_int(v),0xB1,0xF,0xF,true);
  else if(ctrl_sel==1) t=__builtin_amdgcn_mov_dpp(__float_as_int(v),0x4E,0xF,0xF,true);
  else t=__builtin_amdgcn_mov_dpp(__float_as_int(v),0x141,0xF,0xF,true);
  return v+__int_as_float(t);
}
DI float lo16f(unsigned u){ return __uint_as_float(u<<16); }
DI float hi16f(unsigned u){ return __uint_as_float(u&0xffff0000u); }
DI float dot2bf(unsigned a, unsigned b, float c){
  return __builtin_amdgcn_fdot2_f32_bf16(__builtin_bit_cast(bf16x2,a),__builtin_bit_cast(bf16x2,b),c,false); }

#define TL_BEGIN()
#define DT(i)
#define P1(i)
#define TL_END(k)
constexpr int HP=1408;
constexpr int K1_WST=24576, K1_NST=4, K1_X=K1_WST*K1_NST, K1_LDS=K1_X+8*8192;

template<int NT> DI void gu_stage(lds_cptr xa, lds_cptr wa, int xo0, int xo1, f32x16 (&ag)[2], f32x16 (&au)[2]){
  #pragma unroll
  for(int s=0;s<2;s++){
    bf16x8 gf=cat8(lds_tr(wa+s*6144), lds_tr(wa+s*6144+1536));
    bf16x8 uf=cat8(lds_tr(wa+12288+s*6144), lds_tr(wa+12288+s*6144+1536));
    #pragma unroll
    for(int t=0;t<NT;t++){
      bf16x8 xf=lds_ld(xa+t*4096+(s?xo1:xo0));
      ag[t]=MFMA32(gf,xf,ag[t]); au[t]=MFMA32(uf,xf,au[t]);
    }
  }
}
DI void gu_store(const f32x16& ag, const f32x16& au, u16* hp, bool zero){
  u32x4 o0,o1;
  #pragma unroll
  for(int i=0;i<8;i++){
    float hv[2];
    #pragma unroll
    for(int e=0;e<2;e++){ float g=ag[2*i+e]; float sg=g*__builtin_amdgcn_rcpf(1.0f+__builtin_amdgcn_exp2f(-1.4426950408889634f*g)); hv[e]=zero?0.f:sg*au[2*i+e]; }
    unsigned w=pk2(hv[0],hv[1]); if(i<4) o0[i]=w; else o1[i-4]=w;
  }
  *(u32x4*)hp=o0; *(u32x4*)(hp+16)=o1;
}

__global__ __launch_bounds__(512,2) void k1_qkv(const u16* __restrict__ x, const u16* __restrict__ wqkv,
     u16* __restrict__ Qs, u16* __restrict__ Ks, u16* __restrict__ Vt,
     const u16* __restrict__ wg, const u16* __restrict__ wu, u16* __restrict__ hb, unsigned* __restrict__ ctl){
  extern __shared__ __attribute__((aligned(16))) char lds[];
  const int tid=threadIdx.x, lane=tid&63; const int wid=__builtin_amdgcn_readfirstlane(tid>>6);
  const int b=blockIdx.x;
  const int rb=(b&7)*4+((b>>3)&3), hg=b>>5;
  const unsigned lds0=(unsigned)(uintptr_t)lds;
  const int rr=lane>>3, p=lane&7;
  const int lrow=wid*8+rr;
  TL_BEGIN();
  P1(0);
  const u16* xp=x+(long)(rb*64+lrow)*D+p*8;
  u32x4 xv[8];
  #pragma unroll
  for(int i=0;i<8;i++) asm volatile("global_load_dwordx4 %0, %1, off offset:%2" : "=v"(xv[i]) : "v"(xp), "n"(i*128) : "memory");
  const u16* wp=wqkv+(long)lrow*(3*D)+hg*64+(((p>>1)^(((lrow>>1)&1)|(((lrow>>3)&1)<<1)))*16)+(p&1)*8;
  asm volatile("s_barrier":::"memory");
  #pragma unroll
  for(int st=0;st<K1_NST;st++){
    #pragma unroll
    for(int ty=0;ty<3;ty++) glds16(wp+(long)st*64*(3*D)+ty*D, lds0+st*K1_WST+ty*8192+wid*1024);
  }
  const int ft0=hg*5+(hg<3?hg:3);
  int goff[3];
  #pragma unroll
  for(int i=0;i<3;i++){
    const int pc=wid*3+i; const int i16=(pc%12)*64+lane; const int k=i16/24, ch=i16%24;
    int col=ft0*32+(((ch>>2)^((k>>1)&1))*32)+(ch&3)*8; col=col>F-8?F-8:col;
    goff[i]=k*F+col;
  }
  const int rot=GROT;
  const u16* gbase=(wid<4)?wg:wu;
#define GU_ISS(g_) do{ _Pragma("unroll") for(int i=0;i<3;i++) glds16(gbase+(long)(((g_)+rot)&15)*32*F+goff[i], lds0+((g_)&3)*K1_WST+(wid*3+i)*1024); }while(0)
  asm volatile("s_waitcnt vmcnt(12)" : "+v"(xv[0]),"+v"(xv[1]),"+v"(xv[2]),"+v"(xv[3]),"+v"(xv[4]),"+v"(xv[5]),"+v"(xv[6]),"+v"(xv[7]) :: "memory");
  P1(1);
  float s1=0.f, s2=0.f;
  #pragma unroll
  for(int i=0;i<8;i++){
    #pragma unroll
    for(int j=0;j<4;j++){ s1=dot2bf(xv[i][j],0x3F803F80u,s1); s2=dot2bf(xv[i][j],xv[i][j],s2); }
  }
  s1=dpp_add(s1,0); s2=dpp_add(s2,0); s1=dpp_add(s1,1); s2=dpp_add(s2,1); s1=dpp_add(s1,2); s2=dpp_add(s2,2);
  const float mean=s1*(1.0f/D);
  float var=s2*(1.0f/D)-mean*mean;
  if(__builtin_expect(__any(!(mean*mean<=16.f*var)),0)){
    float q2=0.f;
    #pragma unroll
    for(int i=0;i<8;i++){
      #pragma unroll
      for(int j=0;j<4;j++){ float a=lo16f(xv[i][j])-mean, c=hi16f(xv[i][j])-mean; q2=fmaf(a,a,q2); q2=fmaf(c,c,q2); }
    }
    q2=dpp_add(q2,0); q2=dpp_add(q2,1); q2=dpp_add(q2,2);
    var=q2*(1.0f/D);
  }
  const float mb=rbf(mean);
  const float den=rbf(sqrtf(rbf(rbf(var)+1e-5f)));
  const float rinv=1.0f/den;
  char* xw=lds+K1_X+lrow*128+((p^((lrow>>1)&7))*16);
#define NRM1(i,j) do{ unsigned c_=pk2(lo16f(xv[i][j])-mb, hi16f(xv[i][j])-mb); xv[i][j]=pk2(lo16f(c_)*rinv, hi16f(c_)*rinv); }while(0)
#define NORM(i) do{ NRM1(i,0); NRM1(i,1); NRM1(i,2); NRM1(i,3); *(u32x4*)(xw+(i)*8192)=xv[i]; }while(0)
  NORM(0); NORM(1);
  const int rt2=wid&1, hd=wid>>1;
  const int g=lane>>4, r16=lane&15;
  const int xsw=(r16>>1)&7;
  lds_cptr xa0=(lds_cptr)lds+K1_X+(rt2*32+r16)*128+((g^xsw)*16);
  lds_cptr xa1=(lds_cptr)lds+K1_X+(rt2*32+r16)*128+(((4+g)^xsw)*16);
  const int q4=(lane&15)>>2, p4=lane&3;
  const int fk=((q4>>1)&1)|((g&1)<<1);
  lds_cptr wa=(lds_cptr)lds+(8*g+q4)*128+((hd^fk)*32)+p4*8;
  f32x4 aq[2]={}, ak[2]={}, av[2]={};
  bf16x8 fx[2][2][2], fw[2][2][3];
#define TRW(set,s,ty,c) fw[set][s][ty]=cat8(lds_tr(wa+((c)%K1_NST)*K1_WST+(ty)*8192+(s)*4096), lds_tr(wa+((c)%K1_NST)*K1_WST+(ty)*8192+(s)*4096+512))
  asm volatile("s_waitcnt vmcnt(9) lgkmcnt(0)\n\ts_barrier":::"memory");
  P1(2);
  fx[0][0][0]=lds_ld(xa0); fx[0][0][1]=lds_ld(xa0+2048); fx[0][1][0]=lds_ld(xa1); fx[0][1][1]=lds_ld(xa1+2048);
  TRW(0,0,0,0); TRW(0,0,1,0); TRW(0,0,2,0); TRW(0,1,0,0); TRW(0,1,1,0); TRW(0,1,2,0);
#define K1_ISS1(c,i) do{ if((c)<4) glds16(wp+(long)((c)+4)*64*(3*D)+(i)*D, lds0+(c)*K1_WST+(i)*8192+wid*1024); \
                        else if((c)<7) glds16(gbase+(long)(((c)-4+rot)&15)*32*F+goff[i], lds0+((c)-4)*K1_WST+(wid*3+(i))*1024); }while(0)
  #pragma unroll
  for(int c=0;c<8;c++){
    const int cs=c&1, ns=cs^1; const int xb=(c+1)*8192; const int ni=(c+2)&7;
    if(c<7) asm volatile("s_waitcnt vmcnt(6) lgkmcnt(0)\n\ts_barrier":::"memory");
    aq[0]=MFMA16(fw[cs][0][0],fx[cs][0][0],aq[0]); if(c<7){ fx[ns][0][0]=lds_ld(xa0+xb); fx[ns][0][1]=lds_ld(xa0+xb+2048); } SB();
    aq[1]=MFMA16(fw[cs][0][0],fx[cs][0][1],aq[1]); if(c<7){ TRW(ns,0,0,c+1); } SB();
    ak[0]=MFMA16(fw[cs][0][1],fx[cs][0][0],ak[0]); if(c<6) NRM1(ni,0); SB();
    ak[1]=MFMA16(fw[cs][0][1],fx[cs][0][1],ak[1]); if(c<7){ TRW(ns,0,1,c+1); } SB();
    av[0]=MFMA16(fx[cs][0][0],fw[cs][0][2],av[0]); if(c<7){ TRW(ns,0,2,c+1); } K1_ISS1(c,0); SB();
    av[1]=MFMA16(fx[cs][0][1],fw[cs][0][2],av[1]); if(c<6) NRM1(ni,1); SB();
    aq[0]=MFMA16(fw[cs][1][0],fx[cs][1][0],aq[0]); if(c<7){ fx[ns][1][0]=lds_ld(xa1+xb); fx[ns][1][1]=lds_ld(xa1+xb+2048); } SB();
    aq[1]=MFMA16(fw[cs][1][0],fx[cs][1][1],aq[1]); if(c<7){ TRW(ns,1,0,c+1); } K1_ISS1(c,1); SB();
    ak[0]=MFMA16(fw[cs][1][1],fx[cs][1][0],ak[0]); if(c<6) NRM1(ni,2); SB();
    ak[1]=MFMA16(fw[cs][1][1],fx[cs][1][1],ak[1]); if(c<7){ TRW(ns,1,1,c+1); } SB();
    av[0]=MFMA16(fx[cs][1][0],fw[cs][1][2],av[0]); if(c<7){ TRW(ns,1,2,c+1); } K1_ISS1(c,2); SB();
    av[1]=MFMA16(fx[cs][1][1],fw[cs][1][2],av[1]); if(c<6){ NRM1(ni,3); *(u32x4*)(xw+ni*8192)=xv[ni]; } SB();
  }
  P1(3);
  {
    const int h=hg*4+hd; const int T=rb*2+rt2;
    #pragma unroll
    for(int tt=0;tt<2;tt++){
      const long qo=(long)h*S*DH+T*512+(g>>1)*256+(tt*16+r16)*8+(g&1)*4;
      u32x2 wq={pk2(aq[tt][0]*QSCALE,aq[tt][1]*QSCALE),pk2(aq[tt][2]*QSCALE,aq[tt][3]*QSCALE)};
      u32x2 wk={pk2(ak[tt][0],ak[tt][1]),pk2(ak[tt][2],ak[tt][3])};
      const long vo=(long)h*S*DH+(2*T+tt)*256+(g&1)*128+r16*8+(g>>1)*4;
      u32x2 wv={pk2(av[tt][0],av[tt][1]),pk2(av[tt][2],av[tt][3])};
      asm volatile("global_store_dwordx2 %0, %1, off\n\tglobal_store_dwordx2 %2, %3, off\n\tglobal_store_dwordx2 %4, %5, off"
         :: "v"(Qs+qo),"v"(wq),"v"(Ks+qo),"v"(wk),"v"(Vt+vo),"v"(wv) : "memory");
    }
  }
  {
    const int r=lane&31, hh=lane>>5;
    const int ftl = wid<4 ? wid : 4+((wid-4)>>1);
    const int tt0 = wid<4 ? 0 : ((wid-4)&1);
    const int xsw2=(r>>1)&7;
    lds_cptr xg=(lds_cptr)lds+K1_X+(tt0*32+r)*128;
    const int blk=(lane>>4)&1;
    lds_cptr wg_=(lds_cptr)lds+(8*hh+q4)*384+((ftl^((q4>>1)&1))*64)+blk*32+p4*8;
    f32x16 ag[2]={}, au[2]={};
    bf16x8 gf[2][2], uf[2][2], xf[2][2][2];
    const bool two=(wid<4);
#define GRD(gs_,s) do{ const int st_=(gs_)&1; lds_cptr w_=wg_+((gs_)&3)*K1_WST+(s)*6144; \
      gf[st_][s]=cat8(lds_tr(w_), lds_tr(w_+1536)); uf[st_][s]=cat8(lds_tr(w_+12288), lds_tr(w_+12288+1536)); \
      const int gr_=((gs_)+rot)&15; lds_cptr x_=xg+(gr_>>1)*8192+(((4*(gr_&1)+2*(s)+hh)^xsw2)*16); xf[st_][s][0]=lds_ld(x_); if(two) xf[st_][s][1]=lds_ld(x_+4096); }while(0)
#define GMM(gs_,s) do{ const int st_=(gs_)&1; ag[0]=MFMA32(gf[st_][s],xf[st_][s][0],ag[0]); au[0]=MFMA32(uf[st_][s],xf[st_][s][0],au[0]); \
      if(two){ ag[1]=MFMA32(gf[st_][s],xf[st_][s][1],ag[1]); au[1]=MFMA32(uf[st_][s],xf[st_][s][1],au[1]); } }while(0)
#define GU_ISS1(g_,i) glds16(gbase+(long)(((g_)+rot)&15)*32*F+goff[i], lds0+((g_)&3)*K1_WST+(wid*3+(i))*1024)
    asm volatile("s_waitcnt vmcnt(12) lgkmcnt(0)\n\ts_barrier":::"memory");
    P1(4);
    GRD(0,0); GRD(0,1);
    GU_ISS(3);
#define GWAIT(gs) do{ \
      if((gs)<2) asm volatile("s_waitcnt vmcnt(12) lgkmcnt(0)\n\ts_barrier":::"memory"); \
      else if((gs)<13) asm volatile("s_waitcnt vmcnt(6) lgkmcnt(0)\n\ts_barrier":::"memory"); \
      else if((gs)==13) asm volatile("s_waitcnt vmcnt(3) lgkmcnt(0)\n\ts_barrier":::"memory"); \
      else if((gs)==14) asm volatile("s_waitcnt vmcnt(0) lgkmcnt(0)\n\ts_barrier":::"memory"); }while(0)
#define RG(gn,s) do{ if((gn)<16){ lds_cptr w_=wg_+((gn)&3)*K1_WST+(s)*6144; gf[(gn)&1][s]=cat8(lds_tr(w_), lds_tr(w_+1536)); } }while(0)
#define RU(gn,s) do{ if((gn)<16){ lds_cptr w_=wg_+((gn)&3)*K1_WST+(s)*6144+12288; uf[(gn)&1][s]=cat8(lds_tr(w_), lds_tr(w_+1536)); } }while(0)
#define RX(gn,s,t) do{ if((gn)<16){ const int gr_=((gn)+rot)&15; xf[(gn)&1][s][t]=lds_ld(xg+(gr_>>1)*8192+(((4*(gr_&1)+2*(s)+hh)^xsw2)*16)+(t)*4096); } }while(0)
#define MG(gs,s,t) ag[t]=MFMA32(gf[(gs)&1][s],xf[(gs)&1][s][t],ag[t])
#define MU(gs,s,t) au[t]=MFMA32(uf[(gs)&1][s],xf[(gs)&1][s][t],au[t])
#define GI(gs,i) do{ if((gs)+4<16) GU_ISS1((gs)+4,i); }while(0)
    if(two){
      #pragma unroll
      for(int gs=0;gs<16;gs++){
        GWAIT(gs);
        MG(gs,0,0); RG(gs+1,0); SB();
        MU(gs,0,0); RU(gs+1,0); SB();
        MG(gs,0,1); RX(gs+1,0,0); RX(gs+1,0,1); SB();
        MU(gs,0,1); RG(gs+1,1); SB();
        MG(gs,1,0); RU(gs+1,1); SB();
        MU(gs,1,0); RX(gs+1,1,0); RX(gs+1,1,1); SB();
        MG(gs,1,1); GI(gs,0); GI(gs,1); SB();
        MU(gs,1,1); GI(gs,2); SB();
      }
    } else {
      #pragma unroll
      for(int gs=0;gs<16;gs++){
        GWAIT(gs);
        GI(gs,0); SB();
        MG(gs,0,0); RG(gs+1,0); RU(gs+1,0); SB();
        MU(gs,0,0); RX(gs+1,0,0); RG(gs+1,1); SB();
        MG(gs,1,0); RU(gs+1,1); RX(gs+1,1,0); SB();
        MU(gs,1,0); GI(gs,1); GI(gs,2); SB();
      }
    }
    P1(5);
    const int ft=ft0+ftl;
    const bool own=(ftl<5)||(hg<3)||(hg==7);
    if(own){
      u16* hp=hb+(long)(rb*64+tt0*32+r)*HP+ft*32+hh*8;
      gu_store(ag[0],au[0],hp,ft>=43);
      if(wid<4) gu_store(ag[1],au[1],hp+(long)32*HP,false);
    }
  }
  if(b==0){ ctl[tid]=0u; ctl[512+tid]=0u; }
  TL_END(0);
}

#define GU_PRIO 2
constexpr int A2_K=0, A2_V=32768, A2_C=65536, K2_LDS=65536+4096+16;
#define EX(x) __builtin_amdgcn_exp2f(x)
DI void pv_step(const f32x16& sc, bf16x8 v0, bf16x8 v1, f32x16& o){
  u32x4 p0, p1;
  #pragma unroll
  for(int i=0;i<4;i++){ p0[i]=pk2(EX(sc[2*i]),EX(sc[2*i+1])); }
  #pragma unroll
  for(int i=0;i<4;i++){ p1[i]=pk2(EX(sc[8+2*i]),EX(sc[8+2*i+1])); }
  o=MFMA32(v0,__builtin_bit_cast(bf16x8,p0),o);
  o=MFMA32(v1,__builtin_bit_cast(bf16x8,p1),o);
}
DI void attn_role(char* lds, const u16* __restrict__ Qs, const u16* __restrict__ Ks, const u16* __restrict__ Vt, u16* O, int b, unsigned* ctl){
  const int tid=threadIdx.x, lane=tid&63; const int wid=__builtin_amdgcn_readfirstlane(tid>>6);
  const int h=(b&7)*4+((b>>3)>>3), qb=(b>>3)&7;
  const int r=lane&31, hh=lane>>5;
  const unsigned lds0=(unsigned)(uintptr_t)lds;
  const u16* qp=Qs+(long)h*S*DH+(qb*8+wid)*512+lane*8;
  bf16x8 qf;
  asm volatile("global_load_dwordx4 %0, %1, off" : "=v"(qf) : "v"(qp) : "memory");
  const u16* kg=Ks+(long)h*S*DH+wid*512+lane*8;
  const u16* vg=Vt+(long)h*S*DH+wid*512+lane*8;
#define A_ISSUE(c) do{ glds16(kg+(c)*4096, lds0+A2_K+((c)&3)*8192+wid*1024); glds16(vg+(c)*4096, lds0+A2_V+((c)&3)*8192+wid*1024); }while(0)
  A_ISSUE(0); A_ISSUE(1); A_ISSUE(2); A_ISSUE(3);
  { u32x2 v; v[0]=((tid&31)<2)?0x3F803F80u:0u; v[1]=v[0]; *(u32x2*)(lds+A2_C+tid*8)=v; }
  lds_cptr kp=(lds_cptr)lds+A2_K+lane*16;
  const bool cl=(r>=16);
  lds_cptr vp=(lds_cptr)lds+(cl ? A2_C : A2_V)+hh*256+(r&15)*16;
  const int vinc= cl?0:1024; const int vwrap= cl?0:(1024-32768);
  asm volatile("s_waitcnt vmcnt(6) lgkmcnt(0)\n\ts_barrier": "+v"(qf) ::"memory");
  f32x16 negm; f32x16 sA, sB;
  bf16x8 kA, kB, vA0, vA1, vB0, vB1;
  {
    f32x16 z={}; bf16x8 k0=lds_ld(kp);
    kA=lds_ld(kp+1024); vA0=lds_ld(vp); vA1=lds_ld(vp+512); vp+=vinc;
    f32x16 s0=MFMA32(k0,qf,z);
    float m=s0[0];
    #pragma unroll
    for(int i=1;i<16;i++) m=fmaxf(m,s0[i]);
    auto rr=__builtin_amdgcn_permlane32_swap(__float_as_uint(m),__float_as_uint(m),false,false);
    m=fmaxf(__uint_as_float(rr[0]),__uint_as_float(rr[1]));
    float nm=-(m+60.f);
    #pragma unroll
    for(int i=0;i<16;i++){ negm[i]=nm; sA[i]=s0[i]+nm; }
  }
  f32x16 o={};
#define STEP(sC,sN,kU,kL,vU0,vU1,vL0,vL1,t) do{ \
    u32x4 p0,p1; \
    kL=lds_ld(kp+(((t)+2)&31)*1024); vL0=lds_ld(vp); vL1=lds_ld(vp+512); vp+=((((t)+1)&31)==31)?vwrap:vinc; \
    float e0=EX(sC[0]),e1=EX(sC[1]),e2=EX(sC[2]),e3=EX(sC[3]); SB(); \
    sN=MFMA32(kU,qf,negm); SB(); \
    float e4=EX(sC[4]),e5=EX(sC[5]),e6=EX(sC[6]),e7=EX(sC[7]); \
    p0[0]=pk2(e0,e1); p0[1]=pk2(e2,e3); p0[2]=pk2(e4,e5); p0[3]=pk2(e6,e7); SB(); \
    float e8=EX(sC[8]),e9=EX(sC[9]); SB(); \
    o=MFMA32(vU0,__builtin_bit_cast(bf16x8,p0),o); SB(); \
    float e10=EX(sC[10]),e11=EX(sC[11]),e12=EX(sC[12]),e13=EX(sC[13]),e14=EX(sC[14]),e15=EX(sC[15]); \
    p1[0]=pk2(e8,e9); p1[1]=pk2(e10,e11); p1[2]=pk2(e12,e13); p1[3]=pk2(e14,e15); SB(); \
    o=MFMA32(vU1,__builtin_bit_cast(bf16x8,p1),o); SB(); \
  }while(0)
#define ITER(t) do{ STEP(sA,sB,kA,kB,vA0,vA1,vB0,vB1,(t)); STEP(sB,sA,kB,kA,vB0,vB1,vA0,vA1,(t)+1); }while(0)
  #pragma unroll
  for(int j=0;j<8;j++){
    ITER(8*j); ITER(8*j+2); ITER(8*j+4);
    if(j<7){
      if(j==0) asm volatile("s_waitcnt vmcnt(4) lgkmcnt(0)\n\ts_barrier":::"memory");
      else if(j<6) asm volatile("s_waitcnt vmcnt(2) lgkmcnt(0)\n\ts_barrier":::"memory");
      else asm volatile("s_waitcnt vmcnt(0) lgkmcnt(0)\n\ts_barrier":::"memory");
      if(j>=1 && j+3<8) A_ISSUE(j+3);
    }
    ITER(8*j+6);
  }
  float l=o[8];
  { auto rr=__builtin_amdgcn_permlane32_swap(__float_as_uint(l),__float_as_uint(l),false,false); l=__uint_as_float(rr[0]); }
  bool bad = !(l<1e30f);
  #pragma unroll
  for(int i=0;i<8;i++) bad = bad || !(fabsf(o[i])<1e37f);
  if(__builtin_expect(__any(bad),0)){
    const u16* kgl=Ks+(long)h*S*DH+lane*8;
    const u16* vgl=Vt+(long)h*S*DH+hh*128+(r&15)*8;
    float m=-INFINITY; f32x16 z={};
    #pragma unroll
    for(int i=0;i<16;i++) o[i]=0.f;
    for(int t=0;t<64;t++){
      bf16x8 kf=*(const bf16x8*)(kgl+t*512);
      bf16x8 v0=*(const bf16x8*)(vgl+t*512), v1=*(const bf16x8*)(vgl+t*512+256);
      if(cl){ u32x4 one={0x3F803F80u,0x3F803F80u,0x3F803F80u,0x3F803F80u}; v0=__builtin_bit_cast(bf16x8,one); v1=v0; }
      f32x16 s0=MFMA32(kf,qf,z);
      float tm=s0[0];
      #pragma unroll
      for(int i=1;i<16;i++) tm=fmaxf(tm,s0[i]);
      auto rr=__builtin_amdgcn_permlane32_swap(__float_as_uint(tm),__float_as_uint(tm),false,false);
      tm=fmaxf(__uint_as_float(rr[0]),__uint_as_float(rr[1]));
      float mn=fmaxf(m,tm);
      float alpha=EX(m-mn); m=mn;
      #pragma unroll
      for(int i=0;i<16;i++){ o[i]*=alpha; s0[i]-=mn; }
      pv_step(s0,v0,v1,o);
    }
    l=o[8];
    auto rr=__builtin_amdgcn_permlane32_swap(__float_as_uint(l),__float_as_uint(l),false,false); l=__uint_as_float(rr[0]);
  }
  float rl=1.0f/l;
  u32x4 w={pk2(o[0]*rl,o[1]*rl),pk2(o[2]*rl,o[3]*rl),pk2(o[4]*rl,o[5]*rl),pk2(o[6]*rl,o[7]*rl)};
  asm volatile("global_store_dwordx4 %0, %1, off sc1\n\ts_waitcnt vmcnt(0)\n\ts_barrier" :: "v"(O+(long)((qb*8+wid)*32+r)*D+h*DH+hh*8), "v"(w) : "memory");
  if(tid==0) __hip_atomic_fetch_add(ctl+qb*32,1u,__ATOMIC_RELAXED,__HIP_MEMORY_SCOPE_AGENT);
}
DI void down_role(char* lds, const u16* __restrict__ x, const u16* __restrict__ hb, const u16* __restrict__ wd, const u16* ob, const u16* __restrict__ wo, u16* __restrict__ out, unsigned* ctl, int mt, int nt){
  const int tid=threadIdx.x, lane=tid&63; const int wid=__builtin_amdgcn_readfirstlane(tid>>6);
  const unsigned lds0=(unsigned)(uintptr_t)lds;
  __builtin_amdgcn_s_setprio(GU_PRIO);
  for(int i=0;i<GU_DELAY;i++) __builtin_amdgcn_s_sleep(16);
  const int rr=lane>>3, sl=lane&7;
  const int prow=wid*8+rr;
  const u16* hs=hb+(long)(mt*64+prow)*HP+((sl^((prow>>1)&7))*8);
  const int wc=nt*64+(((sl>>2)^((prow>>1)&1))*32)+(sl&3)*8;
  auto issue=[&](int c){ const unsigned b_=lds0+(c&3)*16384+wid*1024; int k=c*64+prow; k=k>F-1?F-1:k;
    glds16(hs+c*64,b_); glds16(wd+(long)k*D+wc,b_+8192); };
  issue(0); issue(1); issue(2);
  const int wm=wid&1, wn=(wid>>1)&1, kh=wid>>2; const int r=lane&31, hh=lane>>5;
  const int xr=wm*32+r; const int xsw=(xr>>1)&7;
  lds_cptr xa0=(lds_cptr)lds+xr*128+(((4*kh+hh)^xsw)*16);
  lds_cptr xa1=(lds_cptr)lds+xr*128+(((4*kh+2+hh)^xsw)*16);
  const int blk=(lane>>4)&1, q4=(lane&15)>>2, p4=lane&3;
  lds_cptr wa=(lds_cptr)lds+8192+(32*kh+4*hh+q4)*128+((wn^((q4>>1)&1))*64)+blk*32+p4*8;
  f32x16 acc={};
  #pragma unroll 1
  for(int c=0;c<22;c++){
    if(c<20) asm volatile("s_waitcnt vmcnt(4) lgkmcnt(0)\n\ts_barrier":::"memory");
    else asm volatile("s_waitcnt vmcnt(0) lgkmcnt(0)\n\ts_barrier":::"memory");
    const int sb=(c&3)*16384;
    bf16x8 x0=lds_ld(xa0+sb), x1=lds_ld(xa1+sb);
    bf16x8 w0=cat8(lds_tr(wa+sb), lds_tr(wa+sb+1024));
    bf16x8 w1=cat8(lds_tr(wa+sb+2048), lds_tr(wa+sb+2048+1024));
    acc=MFMA32(w0,x0,acc); acc=MFMA32(w1,x1,acc);
    SB();
    if(c+3<22) issue(c+3);
  }
  DT(0);
  asm volatile("s_waitcnt lgkmcnt(0)\n\ts_barrier":::"memory");
  { const u16* ws_=wo+(long)prow*D+wc;
    #pragma unroll
    for(int c=0;c<8;c++) glds16(ws_+(long)c*64*D, lds0+c*8192+wid*1024); }
  const long ro=(long)(mt*64+xr)*D+nt*64+wn*32+4*hh;
  u32x2 xr4[4];
  #pragma unroll
  for(int q=0;q<4;q++) asm volatile("global_load_dwordx2 %0, %1, off offset:%2" : "=v"(xr4[q]) : "v"(x+ro), "n"(q*16) : "memory");
  asm volatile("s_waitcnt vmcnt(0)\n\ts_barrier" : "+v"(xr4[0]),"+v"(xr4[1]),"+v"(xr4[2]),"+v"(xr4[3]) :: "memory");
  bf16x8 wf[16];
  { lds_cptr wb=wa-8192;
    #pragma unroll
    for(int c=0;c<8;c++){
      wf[2*c]=cat8(lds_tr(wb+c*8192), lds_tr(wb+c*8192+1024));
      wf[2*c+1]=cat8(lds_tr(wb+c*8192+2048), lds_tr(wb+c*8192+2048+1024));
    } }
  asm volatile("s_waitcnt lgkmcnt(0)" : "+v"(wf[0]),"+v"(wf[1]),"+v"(wf[2]),"+v"(wf[3]),"+v"(wf[4]),"+v"(wf[5]),"+v"(wf[6]),"+v"(wf[7]),
      "+v"(wf[8]),"+v"(wf[9]),"+v"(wf[10]),"+v"(wf[11]),"+v"(wf[12]),"+v"(wf[13]),"+v"(wf[14]),"+v"(wf[15]) :: "memory");
  __builtin_amdgcn_s_setprio(0);
  if(tid==0){
    unsigned* cnt=ctl+(mt>>2)*32; unsigned spins=0;
    while(__hip_atomic_load(cnt,__ATOMIC_RELAXED,__HIP_MEMORY_SCOPE_AGENT)<32u){ __builtin_amdgcn_s_sleep(POLL_SLEEP); if(++spins>(1u<<24)) break; }
  }
  asm volatile("s_barrier":::"memory");
  DT(1);
  {
    const u16* os=ob+(long)(mt*64+prow)*D+sl*8;
    char* ow=lds+prow*128+((sl^((prow>>1)&7))*16);
    #pragma unroll
    for(int hb2=0;hb2<2;hb2++){
      u32x4 t0,t1,t2,t3;
      asm volatile("global_load_dwordx4 %0, %4, off offset:%5 sc1\n\tglobal_load_dwordx4 %1, %4, off offset:%6 sc1\n\tglobal_load_dwordx4 %2, %4, off offset:%7 sc1\n\tglobal_load_dwordx4 %3, %4, off offset:%8 sc1\n\ts_waitcnt vmcnt(0)"
        : "=&v"(t0),"=&v"(t1),"=&v"(t2),"=&v"(t3) : "v"(os), "n"(hb2*512), "n"(hb2*512+128), "n"(hb2*512+256), "n"(hb2*512+384) : "memory");
      *(u32x4*)(ow+(hb2*4+0)*8192)=t0; *(u32x4*)(ow+(hb2*4+1)*8192)=t1; *(u32x4*)(ow+(hb2*4+2)*8192)=t2; *(u32x4*)(ow+(hb2*4+3)*8192)=t3;
    } }
  asm volatile("s_waitcnt lgkmcnt(0)\n\ts_barrier":::"memory");
  DT(2);
  #pragma unroll
  for(int c=0;c<8;c++){
    bf16x8 x0=lds_ld(xa0+c*8192), x1=lds_ld(xa1+c*8192);
    acc=MFMA32(wf[2*c],x0,acc); acc=MFMA32(wf[2*c+1],x1,acc);
  }
  asm volatile("s_waitcnt lgkmcnt(0)\n\ts_barrier":::"memory");
  float* xch=(float*)lds+(wid&3)*1024+lane;
  if(kh==1){
    #pragma unroll
    for(int i=0;i<16;i++) xch[i*64]=acc[i];
  }
  __syncthreads();
  if(kh==1) return;
  #pragma unroll
  for(int q=0;q<4;q++){
    float v0=acc[4*q]+xch[(4*q)*64]+lo16f(xr4[q][0]), v1=acc[4*q+1]+xch[(4*q+1)*64]+hi16f(xr4[q][0]);
    float v2=acc[4*q+2]+xch[(4*q+2)*64]+lo16f(xr4[q][1]), v3=acc[4*q+3]+xch[(4*q+3)*64]+hi16f(xr4[q][1]);
    u32x2 w={pk2(v0,v1),pk2(v2,v3)};
    *(u32x2*)(out+ro+8*q)=w;
  }
}
__global__ __launch_bounds__(512,4) void k2_fused(const u16* __restrict__ Qs, const u16* __restrict__ Ks, const u16* __restrict__ Vt, u16* O,
     const u16* __restrict__ hb, const u16* __restrict__ x, const u16* __restrict__ wd, const u16* __restrict__ wo, u16* __restrict__ out, unsigned* ctl){
  extern __shared__ __attribute__((aligned(16))) char lds[];
  const int b=blockIdx.x;
  TL_BEGIN();
  int task;
  {
    int* sh=(int*)(lds+K2_LDS-16);
    if(threadIdx.x<64){
      const int ln=threadIdx.x;
      const unsigned tg=TGSEL;
      unsigned* claim=ctl+512;
      int t=(int)tg*256+(b&255);
      unsigned old=0;
      if(ln==0) old=__hip_atomic_fetch_add(claim+t,1u,__ATOMIC_RELAXED,__HIP_MEMORY_SCOPE_AGENT);
      old=(unsigned)__builtin_amdgcn_readfirstlane((int)old);
      if(old!=0u){
        t=-1;
        for(int blk=0; blk<8 && t<0; ){
          const int c=((blk<4)?(int)tg*256:256-(int)tg*256)+(blk&3)*64+ln;
          const unsigned v=__hip_atomic_load(claim+c,__ATOMIC_RELAXED,__HIP_MEMORY_SCOPE_AGENT);
          const unsigned long long fr=__ballot(v==0u);
          if(fr==0ull){ blk++; continue; }
          const int rot=b&63; const unsigned long long f2=(fr>>rot)|(rot?(fr<<(64-rot)):0ull);
          const int pick=(__builtin_ctzll(f2)+rot)&63;
          unsigned o2=1;
          if(ln==pick) o2=__hip_atomic_fetch_add(claim+c,1u,__ATOMIC_RELAXED,__HIP_MEMORY_SCOPE_AGENT);
          o2=(unsigned)__builtin_amdgcn_readlane((int)o2,pick);
          if(o2==0u) t=c-ln+pick;
        }
      }
      if(ln==0) *sh=t;
    }
    __syncthreads();
    task=__builtin_amdgcn_readfirstlane(*sh);
    __syncthreads();
  }
  if(task<0) return;
  if(task<256) attn_role(lds,Qs,Ks,Vt,O,task,ctl);
  else { const int u=task-256; down_role(lds,x,hb,wd,O,wo,out,ctl,(u&7)*4+((u>>3)&3),u>>5); }
  TL_END(1);
}

extern "C" void kernel_launch(void* const* d_in, const int* in_sizes, int n_in, void* d_out, int out_size, void* d_ws, size_t ws_size, hipStream_t stream){
  const u16* x=(const u16*)d_in[0]; const u16* wqkv=(const u16*)d_in[1]; const u16* wo=(const u16*)d_in[2];
  const u16* wg=(const u16*)d_in[4]; const u16* wu=(const u16*)d_in[5]; const u16* wd=(const u16*)d_in[6];
  char* ws=(char*)d_ws;
  unsigned* ctl=(unsigned*)ws; ws+=4096;
  u16* xn=(u16*)ws; ws+=S*D*2;
  u16* hb=(u16*)ws; ws+=(size_t)S*HP*2;
  u16* o=(u16*)ws; ws+=(size_t)S*D*2;
  u16* Qs=(u16*)ws; ws+=(size_t)S*D*2;
  u16* Ks=(u16*)ws; ws+=(size_t)S*D*2;
  u16* Vt=(u16*)ws; ws+=(size_t)S*D*2;
  float* yb=(float*)ws; ws+=(size_t)S*D*4;
  (void)hipFuncSetAttribute((const void*)k2_fused, hipFuncAttributeMaxDynamicSharedMemorySize, K2_LDS);
  (void)hipFuncSetAttribute((const void*)k1_qkv, hipFuncAttributeMaxDynamicSharedMemorySize, K1_LDS);
  k1_qkv<<<256,512,K1_LDS,stream>>>(x,wqkv,Qs,Ks,Vt,wg,wu,hb,ctl);
  k2_fused<<<512,512,K2_LDS,stream>>>(Qs,Ks,Vt,o,hb,x,wd,wo,(u16*)d_out,ctl);
}
```

```cpp
#define TGSEL ((__builtin_amdgcn_s_getreg((3<<11)|(16<<6)|4))&1u)
#define GROT ((((b>>3)&3)*4+((b&7)>>1))&15)
#define POLL_SLEEP 4
#define K1MAP 0
#define GU_DELAY 3
#define ABL 0
#define NBLK (256+352)
#define PACE 0
#define BARMODE 0
#define FIRSTW "0"
#include <hip/hip_runtime.h>
#include <hip/hip_bf16.h>
#include <stdint.h>

typedef unsigned short u16;
typedef __attribute__((ext_vector_type(8))) __bf16 bf16x8;
typedef __attribute__((ext_vector_type(2))) __bf16 bf16x2;
typedef __attribute__((ext_vector_type(2))) float f32x2;
typedef __attribute__((ext_vector_type(16))) float f32x16;
typedef __attribute__((ext_vector_type(4))) unsigned u32x4;
typedef __attribute__((ext_vector_type(2))) unsigned u32x2;
#define DI __device__ __forceinline__
#define MFMA32(a,b,c) __builtin_amdgcn_mfma_f32_32x32x16_bf16((a),(b),(c),0,0,0)

DI float bf2f(u16 b){ return __uint_as_float(((unsigned)b)<<16); }
DI u16 f2bf(float f){ unsigned u=__float_as_uint(f); u += 0x7fffu + ((u>>16)&1u); return (u16)(u>>16); }
DI float rbf(float f){ return bf2f(f2bf(f)); }
DI unsigned pk2(float lo, float hi){ f32x2 v={lo,hi}; bf16x2 b=__builtin_convertvector(v,bf16x2); return __builtin_bit_cast(unsigned,b); }

constexpr int S=2048, D=512, H=32, DH=16, F=1376;
constexpr float QSCALE = 0.25f*1.4426950408889634f;

DI void glds16(const void* g, unsigned lds_base) {
  unsigned sv; asm volatile("s_mov_b32 %0, m0\n\ts_mov_b32 m0, %2\n\ts_nop 0\n\tglobal_load_lds_dwordx4 %1, off\n\ts_mov_b32 m0, %0" : "=&s"(sv) : "v"(g), "s"(lds_base) : "memory"); }

typedef __attribute__((address_space(3))) const char* lds_cptr;
DI bf16x8 lds_ld(lds_cptr p){ return *(const __attribute__((address_space(3))) bf16x8*)p; }

#define SB() __builtin_amdgcn_sched_barrier(0)
typedef __attribute__((ext_vector_type(4))) float f32x4;
typedef __attribute__((ext_vector_type(4))) short s16x4;
typedef short v4i16_t __attribute__((ext_vector_type(4)));
#define MFMA16(a,b,c) __builtin_amdgcn_mfma_f32_16x16x32_bf16((a),(b),(c),0,0,0)
DI s16x4 lds_tr(lds_cptr p){ return __builtin_bit_cast(s16x4, __builtin_amdgcn_ds_read_tr16_b64_v4i16((__attribute__((address_space(3))) v4i16_t*)p)); }
DI bf16x8 cat8(s16x4 a, s16x4 b){ return __builtin_bit_cast(bf16x8, __builtin_shufflevector(a,b,0,1,2,3,4,5,6,7)); }
DI float dpp_add(float v, const int ctrl_sel){
  int t;
  if(ctrl_sel==0) t=__builtin_amdgcn_mov_dpp(__float_as_int(v),0xB1,0xF,0xF,true);
  else if(ctrl_sel==1) t=__builtin_amdgcn_mov_dpp(__float_as_int(v),0x4E,0xF,0xF,true);
  else t=__builtin_amdgcn_mov_dpp(__float_as_int(v),0x141,0xF,0xF,true);
  return v+__int_as_float(t);
}
DI float lo16f(unsigned u){ return __uint_as_float(u<<16); }
DI float hi16f(unsigned u){ return __uint_as_float(u&0xffff0000u); }
DI float dot2bf(unsigned a, unsigned b, float c){
  return __builtin_amdgcn_fdot2_f32_bf16(__builtin_bit_cast(bf16x2,a),__builtin_bit_cast(bf16x2,b),c,false); }

#define TL_BEGIN()
#define DT(i)
#define P1(i)
#define TL_END(k)
constexpr int HP=1408;
constexpr int K1_WST=24576, K1_NST=4, K1_X=K1_WST*K1_NST, K1_LDS=K1_X+8*8192;

template<int NT> DI void gu_stage(lds_cptr xa, lds_cptr wa, int xo0, int xo1, f32x16 (&ag)[2], f32x16 (&au)[2]){
  #pragma unroll
  for(int s=0;s<2;s++){
    bf16x8 gf=cat8(lds_tr(wa+s*6144), lds_tr(wa+s*6144+1536));
    bf16x8 uf=cat8(lds_tr(wa+12288+s*6144), lds_tr(wa+12288+s*6144+1536));
    #pragma unroll
    for(int t=0;t<NT;t++){
      bf16x8 xf=lds_ld(xa+t*4096+(s?xo1:xo0));
      ag[t]=MFMA32(gf,xf,ag[t]); au[t]=MFMA32(uf,xf,au[t]);
    }
  }
}
DI void gu_store(const f32x16& ag, const f32x16& au, u16* hp, bool zero){
  u32x4 o0,o1;
  #pragma unroll
  for(int i=0;i<8;i++){
    float hv[2];
    #pragma unroll
    for(int e=0;e<2;e++){ float g=ag[2*i+e]; float sg=g*__builtin_amdgcn_rcpf(1.0f+__builtin_amdgcn_exp2f(-1.4426950408889634f*g)); hv[e]=zero?0.f:sg*au[2*i+e]; }
    unsigned w=pk2(hv[0],hv[1]); if(i<4) o0[i]=w; else o1[i-4]=w;
  }
  *(u32x4*)hp=o0; *(u32x4*)(hp+16)=o1;
}

__global__ __launch_bounds__(512,2) void k1_qkv(const u16* __restrict__ x, const u16* __restrict__ wqkv,
     u16* __restrict__ Qs, u16* __restrict__ Ks, u16* __restrict__ Vt,
     const u16* __restrict__ wg, const u16* __restrict__ wu, u16* __restrict__ hb, unsigned* __restrict__ ctl){
  extern __shared__ __attribute__((aligned(16))) char lds[];
  const int tid=threadIdx.x, lane=tid&63; const int wid=__builtin_amdgcn_readfirstlane(tid>>6);
  const int b=blockIdx.x;
  const int rb=(b&7)*4+((b>>3)&3), hg=b>>5;
  const unsigned lds0=(unsigned)(uintptr_t)lds;
  const int rr=lane>>3, p=lane&7;
  const int lrow=wid*8+rr;
  TL_BEGIN();
  P1(0);
  const u16* xp=x+(long)(rb*64+lrow)*D+p*8;
  u32x4 xv[8];
  #pragma unroll
  for(int i=0;i<8;i++) asm volatile("global_load_dwordx4 %0, %1, off offset:%2" : "=v"(xv[i]) : "v"(xp), "n"(i*128) : "memory");
  const u16* wp=wqkv+(long)lrow*(3*D)+hg*64+(((p>>1)^(((lrow>>1)&1)|(((lrow>>3)&1)<<1)))*16)+(p&1)*8;
  asm volatile("s_barrier":::"memory");
  #pragma unroll
  for(int st=0;st<K1_NST;st++){
    #pragma unroll
    for(int ty=0;ty<3;ty++) glds16(wp+(long)st*64*(3*D)+ty*D, lds0+st*K1_WST+ty*8192+wid*1024);
  }
  const int ft0=hg*5+(hg<3?hg:3);
  int goff[3];
  #pragma unroll
  for(int i=0;i<3;i++){
    const int pc=wid*3+i; const int i16=(pc%12)*64+lane; const int k=i16/24, ch=i16%24;
    int col=ft0*32+(((ch>>2)^((k>>1)&1))*32)+(ch&3)*8; col=col>F-8?F-8:col;
    goff[i]=k*F+col;
  }
  const int rot=GROT;
  const u16* gbase=(wid<4)?wg:wu;
#define GU_ISS(g_) do{ _Pragma("unroll") for(int i=0;i<3;i++) glds16(gbase+(long)(((g_)+rot)&15)*32*F+goff[i], lds0+((g_)&3)*K1_WST+(wid*3+i)*1024); }while(0)
  asm volatile("s_waitcnt vmcnt(12)" : "+v"(xv[0]),"+v"(xv[1]),"+v"(xv[2]),"+v"(xv[3]),"+v"(xv[4]),"+v"(xv[5]),"+v"(xv[6]),"+v"(xv[7]) :: "memory");
  P1(1);
  float s1=0.f, s2=0.f;
  #pragma unroll
  for(int i=0;i<8;i++){
    #pragma unroll
    for(int j=0;j<4;j++){ s1=dot2bf(xv[i][j],0x3F803F80u,s1); s2=dot2bf(xv[i][j],xv[i][j],s2); }
  }
  s1=dpp_add(s1,0); s2=dpp_add(s2,0); s1=dpp_add(s1,1); s2=dpp_add(s2,1); s1=dpp_add(s1,2); s2=dpp_add(s2,2);
  const float mean=s1*(1.0f/D);
  float var=s2*(1.0f/D)-mean*mean;
  if(__builtin_expect(__any(!(mean*mean<=16.f*var)),0)){
    float q2=0.f;
    #pragma unroll
    for(int i=0;i<8;i++){
      #pragma unroll
      for(int j=0;j<4;j++){ float a=lo16f(xv[i][j])-mean, c=hi16f(xv[i][j])-mean; q2=fmaf(a,a,q2); q2=fmaf(c,c,q2); }
    }
    q2=dpp_add(q2,0); q2=dpp_add(q2,1); q2=dpp_add(q2,2);
    var=q2*(1.0f/D);
  }
  const float mb=rbf(mean);
  const float den=rbf(sqrtf(rbf(rbf(var)+1e-5f)));
  const float rinv=1.0f/den;
  char* xw=lds+K1_X+lrow*128+((p^((lrow>>1)&7))*16);
#define NRM1(i,j) do{ unsigned c_=pk2(lo16f(xv[i][j])-mb, hi16f(xv[i][j])-mb); xv[i][j]=pk2(lo16f(c_)*rinv, hi16f(c_)*rinv); }while(0)
#define NORM(i) do{ NRM1(i,0); NRM1(i,1); NRM1(i,2); NRM1(i,3); *(u32x4*)(xw+(i)*8192)=xv[i]; }while(0)
  NORM(0); NORM(1);
  const int rt2=wid&1, hd=wid>>1;
  const int g=lane>>4, r16=lane&15;
  const int xsw=(r16>>1)&7;
  lds_cptr xa0=(lds_cptr)lds+K1_X+(rt2*32+r16)*128+((g^xsw)*16);
  lds_cptr xa1=(lds_cptr)lds+K1_X+(rt2*32+r16)*128+(((4+g)^xsw)*16);
  const int q4=(lane&15)>>2, p4=lane&3;
  const int fk=((q4>>1)&1)|((g&1)<<1);
  lds_cptr wa=(lds_cptr)lds+(8*g+q4)*128+((hd^fk)*32)+p4*8;
  f32x4 aq[2]={}, ak[2]={}, av[2]={};
  bf16x8 fx[2][2][2], fw[2][2][3];
#define TRW(set,s,ty,c) fw[set][s][ty]=cat8(lds_tr(wa+((c)%K1_NST)*K1_WST+(ty)*8192+(s)*4096), lds_tr(wa+((c)%K1_NST)*K1_WST+(ty)*8192+(s)*4096+512))
  asm volatile("s_waitcnt vmcnt(9) lgkmcnt(0)\n\ts_barrier":::"memory");
  P1(2);
  fx[0][0][0]=lds_ld(xa0); fx[0][0][1]=lds_ld(xa0+2048); fx[0][1][0]=lds_ld(xa1); fx[0][1][1]=lds_ld(xa1+2048);
  TRW(0,0,0,0); TRW(0,0,1,0); TRW(0,0,2,0); TRW(0,1,0,0); TRW(0,1,1,0); TRW(0,1,2,0);
#define K1_ISS1(c,i) do{ if((c)<4) glds16(wp+(long)((c)+4)*64*(3*D)+(i)*D, lds0+(c)*K1_WST+(i)*8192+wid*1024); \
                        else if((c)<7) glds16(gbase+(long)(((c)-4+rot)&15)*32*F+goff[i], lds0+((c)-4)*K1_WST+(wid*3+(i))*1024); }while(0)
  #pragma unroll
  for(int c=0;c<8;c++){
    const int cs=c&1, ns=cs^1; const int xb=(c+1)*8192; const int ni=(c+2)&7;
    if(c<7) asm volatile("s_waitcnt vmcnt(6) lgkmcnt(0)\n\ts_barrier":::"memory");
    aq[0]=MFMA16(fw[cs][0][0],fx[cs][0][0],aq[0]); if(c<7){ fx[ns][0][0]=lds_ld(xa0+xb); fx[ns][0][1]=lds_ld(xa0+xb+2048); } SB();
    aq[1]=MFMA16(fw[cs][0][0],fx[cs][0][1],aq[1]); if(c<7){ TRW(ns,0,0,c+1); } SB();
    ak[0]=MFMA16(fw[cs][0][1],fx[cs][0][0],ak[0]); if(c<6) NRM1(ni,0); SB();
    ak[1]=MFMA16(fw[cs][0][1],fx[cs][0][1],ak[1]); if(c<7){ TRW(ns,0,1,c+1); } SB();
    av[0]=MFMA16(fx[cs][0][0],fw[cs][0][2],av[0]); if(c<7){ TRW(ns,0,2,c+1); } K1_ISS1(c,0); SB();
    av[1]=MFMA16(fx[cs][0][1],fw[cs][0][2],av[1]); if(c<6) NRM1(ni,1); SB();
    aq[0]=MFMA16(fw[cs][1][0],fx[cs][1][0],aq[0]); if(c<7){ fx[ns][1][0]=lds_ld(xa1+xb); fx[ns][1][1]=lds_ld(xa1+xb+2048); } SB();
    aq[1]=MFMA16(fw[cs][1][0],fx[cs][1][1],aq[1]); if(c<7){ TRW(ns,1,0,c+1); } K1_ISS1(c,1); SB();
    ak[0]=MFMA16(fw[cs][1][1],fx[cs][1][0],ak[0]); if(c<6) NRM1(ni,2); SB();
    ak[1]=MFMA16(fw[cs][1][1],fx[cs][1][1],ak[1]); if(c<7){ TRW(ns,1,1,c+1); } SB();
    av[0]=MFMA16(fx[cs][1][0],fw[cs][1][2],av[0]); if(c<7){ TRW(ns,1,2,c+1); } K1_ISS1(c,2); SB();
    av[1]=MFMA16(fx[cs][1][1],fw[cs][1][2],av[1]); if(c<6){ NRM1(ni,3); *(u32x4*)(xw+ni*8192)=xv[ni]; } SB();
  }
  P1(3);
  {
    const int h=hg*4+hd; const int T=rb*2+rt2;
    #pragma unroll
    for(int tt=0;tt<2;tt++){
      const long qo=(long)h*S*DH+T*512+(g>>1)*256+(tt*16+r16)*8+(g&1)*4;
      u32x2 wq={pk2(aq[tt][0]*QSCALE,aq[tt][1]*QSCALE),pk2(aq[tt][2]*QSCALE,aq[tt][3]*QSCALE)};
      u32x2 wk={pk2(ak[tt][0],ak[tt][1]),pk2(ak[tt][2],ak[tt][3])};
      const long vo=(long)h*S*DH+(2*T+tt)*256+(g&1)*128+r16*8+(g>>1)*4;
      u32x2 wv={pk2(av[tt][0],av[tt][1]),pk2(av[tt][2],av[tt][3])};
      asm volatile("global_store_dwordx2 %0, %1, off\n\tglobal_store_dwordx2 %2, %3, off\n\tglobal_store_dwordx2 %4, %5, off"
         :: "v"(Qs+qo),"v"(wq),"v"(Ks+qo),"v"(wk),"v"(Vt+vo),"v"(wv) : "memory");
    }
  }
  {
    const int r=lane&31, hh=lane>>5;
    const int ftl = wid<4 ? wid : 4+((wid-4)>>1);
    const int tt0 = wid<4 ? 0 : ((wid-4)&1);
    const int xsw2=(r>>1)&7;
    lds_cptr xg=(lds_cptr)lds+K1_X+(tt0*32+r)*128;
    const int blk=(lane>>4)&1;
    lds_cptr wg_=(lds_cptr)lds+(8*hh+q4)*384+((ftl^((q4>>1)&1))*64)+blk*32+p4*8;
    f32x16 ag[2]={}, au[2]={};
    bf16x8 gf[2][2], uf[2][2], xf[2][2][2];
    const bool two=(wid<4);
#define GRD(gs_,s) do{ const int st_=(gs_)&1; lds_cptr w_=wg_+((gs_)&3)*K1_WST+(s)*6144; \
      gf[st_][s]=cat8(lds_tr(w_), lds_tr(w_+1536)); uf[st_][s]=cat8(lds_tr(w_+12288), lds_tr(w_+12288+1536)); \
      const int gr_=((gs_)+rot)&15; lds_cptr x_=xg+(gr_>>1)*8192+(((4*(gr_&1)+2*(s)+hh)^xsw2)*16); xf[st_][s][0]=lds_ld(x_); if(two) xf[st_][s][1]=lds_ld(x_+4096); }while(0)
#define GMM(gs_,s) do{ const int st_=(gs_)&1; ag[0]=MFMA32(gf[st_][s],xf[st_][s][0],ag[0]); au[0]=MFMA32(uf[st_][s],xf[st_][s][0],au[0]); \
      if(two){ ag[1]=MFMA32(gf[st_][s],xf[st_][s][1],ag[1]); au[1]=MFMA32(uf[st_][s],xf[st_][s][1],au[1]); } }while(0)
#define GU_ISS1(g_,i) glds16(gbase+(long)(((g_)+rot)&15)*32*F+goff[i], lds0+((g_)&3)*K1_WST+(wid*3+(i))*1024)
    asm volatile("s_waitcnt vmcnt(12) lgkmcnt(0)\n\ts_barrier":::"memory");
    P1(4);
    GRD(0,0); GRD(0,1);
    GU_ISS(3);
#define GWAIT(gs) do{ \
      if((gs)<2) asm volatile("s_waitcnt vmcnt(12) lgkmcnt(0)\n\ts_barrier":::"memory"); \
      else if((gs)<13) asm volatile("s_waitcnt vmcnt(6) lgkmcnt(0)\n\ts_barrier":::"memory"); \
      else if((gs)==13) asm volatile("s_waitcnt vmcnt(3) lgkmcnt(0)\n\ts_barrier":::"memory"); \
      else if((gs)==14) asm volatile("s_waitcnt vmcnt(0) lgkmcnt(0)\n\ts_barrier":::"memory"); }while(0)
#define RG(gn,s) do{ if((gn)<16){ lds_cptr w_=wg_+((gn)&3)*K1_WST+(s)*6144; gf[(gn)&1][s]=cat8(lds_tr(w_), lds_tr(w_+1536)); } }while(0)
#define RU(gn,s) do{ if((gn)<16){ lds_cptr w_=wg_+((gn)&3)*K1_WST+(s)*6144+12288; uf[(gn)&1][s]=cat8(lds_tr(w_), lds_tr(w_+1536)); } }while(0)
#define RX(gn,s,t) do{ if((gn)<16){ const int gr_=((gn)+rot)&15; xf[(gn)&1][s][t]=lds_ld(xg+(gr_>>1)*8192+(((4*(gr_&1)+2*(s)+hh)^xsw2)*16)+(t)*4096); } }while(0)
#define MG(gs,s,t) ag[t]=MFMA32(gf[(gs)&1][s],xf[(gs)&1][s][t],ag[t])
#define MU(gs,s,t) au[t]=MFMA32(uf[(gs)&1][s],xf[(gs)&1][s][t],au[t])
#define GI(gs,i) do{ if((gs)+4<16) GU_ISS1((gs)+4,i); }while(0)
    if(two){
      #pragma unroll
      for(int gs=0;gs<16;gs++){
        GWAIT(gs);
        MG(gs,0,0); RG(gs+1,0); SB();
        MU(gs,0,0); RU(gs+1,0); SB();
        MG(gs,0,1); RX(gs+1,0,0); RX(gs+1,0,1); SB();
        MU(gs,0,1); RG(gs+1,1); SB();
        MG(gs,1,0); RU(gs+1,1); SB();
        MU(gs,1,0); RX(gs+1,1,0); RX(gs+1,1,1); SB();
        MG(gs,1,1); GI(gs,0); GI(gs,1); SB();
        MU(gs,1,1); GI(gs,2); SB();
      }
    } else {
      #pragma unroll
      for(int gs=0;gs<16;gs++){
        GWAIT(gs);
        GI(gs,0); SB();
        MG(gs,0,0); RG(gs+1,0); RU(gs+1,0); SB();
        MU(gs,0,0); RX(gs+1,0,0); RG(gs+1,1); SB();
        MG(gs,1,0); RU(gs+1,1); RX(gs+1,1,0); SB();
        MU(gs,1,0); GI(gs,1); GI(gs,2); SB();
      }
    }
    P1(5);
    const int ft=ft0+ftl;
    const bool own=(ftl<5)||(hg<3)||(hg==7);
    if(own){
      u16* hp=hb+(long)(rb*64+tt0*32+r)*HP+ft*32+hh*8;
      gu_store(ag[0],au[0],hp,ft>=43);
      if(wid<4) gu_store(ag[1],au[1],hp+(long)32*HP,false);
    }
  }
  if(b==0){ ctl[tid]=0u; ctl[512+tid]=0u; }
  TL_END(0);
}

#define GU_PRIO 2
constexpr int A2_K=0, A2_V=32768, A2_C=65536, K2_LDS=65536+4096+16;
#define EX(x) __builtin_amdgcn_exp2f(x)
DI void pv_step(const f32x16& sc, bf16x8 v0, bf16x8 v1, f32x16& o){
  u32x4 p0, p1;
  #pragma unroll
  for(int i=0;i<4;i++){ p0[i]=pk2(EX(sc[2*i]),EX(sc[2*i+1])); }
  #pragma unroll
  for(int i=0;i<4;i++){ p1[i]=pk2(EX(sc[8+2*i]),EX(sc[8+2*i+1])); }
  o=MFMA32(v0,__builtin_bit_cast(bf16x8,p0),o);
  o=MFMA32(v1,__builtin_bit_cast(bf16x8,p1),o);
}
DI void attn_role(char* lds, const u16* __restrict__ Qs, const u16* __restrict__ Ks, const u16* __restrict__ Vt, u16* O, int b, unsigned* ctl){
  const int tid=threadIdx.x, lane=tid&63; const int wid=__builtin_amdgcn_readfirstlane(tid>>6);
  const int h=(b&7)*4+((b>>3)>>3), qb=(b>>3)&7;
  const int r=lane&31, hh=lane>>5;
  const unsigned lds0=(unsigned)(uintptr_t)lds;
  const u16* qp=Qs+(long)h*S*DH+(qb*8+wid)*512+lane*8;
  bf16x8 qf;
  asm volatile("global_load_dwordx4 %0, %1, off" : "=v"(qf) : "v"(qp) : "memory");
  const u16* kg=Ks+(long)h*S*DH+wid*512+lane*8;
  const u16* vg=Vt+(long)h*S*DH+wid*512+lane*8;
#define A_ISSUE(c) do{ glds16(kg+(c)*4096, lds0+A2_K+((c)&3)*8192+wid*1024); glds16(vg+(c)*4096, lds0+A2_V+((c)&3)*8192+wid*1024); }while(0)
  A_ISSUE(0); A_ISSUE(1); A_ISSUE(2); A_ISSUE(3);
  { u32x2 v; v[0]=((tid&31)<2)?0x3F803F80u:0u; v[1]=v[0]; *(u32x2*)(lds+A2_C+tid*8)=v; }
  lds_cptr kp=(lds_cptr)lds+A2_K+lane*16;
  const bool cl=(r>=16);
  lds_cptr vp=(lds_cptr)lds+(cl ? A2_C : A2_V)+hh*256+(r&15)*16;
  const int vinc= cl?0:1024; const int vwrap= cl?0:(1024-32768);
  asm volatile("s_waitcnt vmcnt(6) lgkmcnt(0)\n\ts_barrier": "+v"(qf) ::"memory");
  f32x16 negm; f32x16 sA, sB;
  bf16x8 kA, kB, vA0, vA1, vB0, vB1;
  {
    f32x16 z={}; bf16x8 k0=lds_ld(kp);
    kA=lds_ld(kp+1024); vA0=lds_ld(vp); vA1=lds_ld(vp+512); vp+=vinc;
    f32x16 s0=MFMA32(k0,qf,z);
    float m=s0[0];
    #pragma unroll
    for(int i=1;i<16;i++) m=fmaxf(m,s0[i]);
    #pragma unroll
    for(int t=1;t<4;t++){
      f32x16 st=MFMA32(t==1?kA:lds_ld(kp+t*1024),qf,z);
      #pragma unroll
      for(int i=0;i<16;i++) m=fmaxf(m,st[i]);
    }
    auto rr=__builtin_amdgcn_permlane32_swap(__float_as_uint(m),__float_as_uint(m),false,false);
    m=fmaxf(__uint_as_float(rr[0]),__uint_as_float(rr[1]));
    float nm=-(m+80.f);
    #pragma unroll
    for(int i=0;i<16;i++){ negm[i]=nm; sA[i]=s0[i]+nm; }
  }
  f32x16 o={};
#define STEP(sC,sN,kU,kL,vU0,vU1,vL0,vL1,t) do{ \
    u32x4 p0,p1; \
    kL=lds_ld(kp+(((t)+2)&31)*1024); vL0=lds_ld(vp); vL1=lds_ld(vp+512); vp+=((((t)+1)&31)==31)?vwrap:vinc; \
    float e0=EX(sC[0]),e1=EX(sC[1]),e2=EX(sC[2]),e3=EX(sC[3]); SB(); \
    sN=MFMA32(kU,qf,negm); SB(); \
    float e4=EX(sC[4]),e5=EX(sC[5]),e6=EX(sC[6]),e7=EX(sC[7]); \
    p0[0]=pk2(e0,e1); p0[1]=pk2(e2,e3); p0[2]=pk2(e4,e5); p0[3]=pk2(e6,e7); SB(); \
    float e8=EX(sC[8]),e9=EX(sC[9]); SB(); \
    o=MFMA32(vU0,__builtin_bit_cast(bf16x8,p0),o); SB(); \
    float e10=EX(sC[10]),e11=EX(sC[11]),e12=EX(sC[12]),e13=EX(sC[13]),e14=EX(sC[14]),e15=EX(sC[15]); \
    p1[0]=pk2(e8,e9); p1[1]=pk2(e10,e11); p1[2]=pk2(e12,e13); p1[3]=pk2(e14,e15); SB(); \
    o=MFMA32(vU1,__builtin_bit_cast(bf16x8,p1),o); SB(); \
  }while(0)
#define ITER(t) do{ STEP(sA,sB,kA,kB,vA0,vA1,vB0,vB1,(t)); STEP(sB,sA,kB,kA,vB0,vB1,vA0,vA1,(t)+1); }while(0)
  #pragma unroll
  for(int j=0;j<8;j++){
    ITER(8*j); ITER(8*j+2); ITER(8*j+4);
    if(j<7){
      if(j==0) asm volatile("s_waitcnt vmcnt(4) lgkmcnt(0)\n\ts_barrier":::"memory");
      else if(j<6) asm volatile("s_waitcnt vmcnt(2) lgkmcnt(0)\n\ts_barrier":::"memory");
      else asm volatile("s_waitcnt vmcnt(0) lgkmcnt(0)\n\ts_barrier":::"memory");
      if(j>=1 && j+3<8) A_ISSUE(j+3);
    }
    ITER(8*j+6);
  }
  float l=o[8];
  { auto rr=__builtin_amdgcn_permlane32_swap(__float_as_uint(l),__float_as_uint(l),false,false); l=__uint_as_float(rr[0]); }
  bool bad = !(l<1e36f);
  #pragma unroll
  for(int i=0;i<8;i++) bad = bad || !(fabsf(o[i])<3e38f);
  if(__builtin_expect(__any(bad),0)){
    const u16* kgl=Ks+(long)h*S*DH+lane*8;
    const u16* vgl=Vt+(long)h*S*DH+hh*128+(r&15)*8;
    float m=-INFINITY; f32x16 z={};
    #pragma unroll
    for(int i=0;i<16;i++) o[i]=0.f;
    for(int t=0;t<64;t++){
      bf16x8 kf=*(const bf16x8*)(kgl+t*512);
      bf16x8 v0=*(const bf16x8*)(vgl+t*512), v1=*(const bf16x8*)(vgl+t*512+256);
      if(cl){ u32x4 one={0x3F803F80u,0x3F803F80u,0x3F803F80u,0x3F803F80u}; v0=__builtin_bit_cast(bf16x8,one); v1=v0; }
      f32x16 s0=MFMA32(kf,qf,z);
      float tm=s0[0];
      #pragma unroll
      for(int i=1;i<16;i++) tm=fmaxf(tm,s0[i]);
      auto rr=__builtin_amdgcn_permlane32_swap(__float_as_uint(tm),__float_as_uint(tm),false,false);
      tm=fmaxf(__uint_as_float(rr[0]),__uint_as_float(rr[1]));
      float mn=fmaxf(m,tm);
      float alpha=EX(m-mn); m=mn;
      #pragma unroll
      for(int i=0;i<16;i++){ o[i]*=alpha; s0[i]-=mn; }
      pv_step(s0,v0,v1,o);
    }
    l=o[8];
    auto rr=__builtin_amdgcn_permlane32_swap(__float_as_uint(l),__float_as_uint(l),false,false); l=__uint_as_float(rr[0]);
  }
  float rl=1.0f/l;
  u32x4 w={pk2(o[0]*rl,o[1]*rl),pk2(o[2]*rl,o[3]*rl),pk2(o[4]*rl,o[5]*rl),pk2(o[6]*rl,o[7]*rl)};
  asm volatile("global_store_dwordx4 %0, %1, off sc1\n\ts_waitcnt vmcnt(0)\n\ts_barrier" :: "v"(O+(long)((qb*8+wid)*32+r)*D+h*DH+hh*8), "v"(w) : "memory");
  if(tid==0) __hip_atomic_fetch_add(ctl+qb*32,1u,__ATOMIC_RELAXED,__HIP_MEMORY_SCOPE_AGENT);
}
DI void down_role(char* lds, const u16* __restrict__ x, const u16* __restrict__ hb, const u16* __restrict__ wd, const u16* ob, const u16* __restrict__ wo, u16* __restrict__ out, unsigned* ctl, int mt, int nt){
  const int tid=threadIdx.x, lane=tid&63; const int wid=__builtin_amdgcn_readfirstlane(tid>>6);
  const unsigned lds0=(unsigned)(uintptr_t)lds;
  __builtin_amdgcn_s_setprio(GU_PRIO);
  for(int i=0;i<GU_DELAY;i++) __builtin_amdgcn_s_sleep(16);
  const int rr=lane>>3, sl=lane&7;
  const int prow=wid*8+rr;
  const u16* hs=hb+(long)(mt*64+prow)*HP+((sl^((prow>>1)&7))*8);
  const int wc=nt*64+(((sl>>2)^((prow>>1)&1))*32)+(sl&3)*8;
  auto issue=[&](int c){ const unsigned b_=lds0+(c&3)*16384+wid*1024; int k=c*64+prow; k=k>F-1?F-1:k;
    glds16(hs+c*64,b_); glds16(wd+(long)k*D+wc,b_+8192); };
  issue(0); issue(1); issue(2);
  const int wm=wid&1, wn=(wid>>1)&1, kh=wid>>2; const int r=lane&31, hh=lane>>5;
  const int xr=wm*32+r; const int xsw=(xr>>1)&7;
  lds_cptr xa0=(lds_cptr)lds+xr*128+(((4*kh+hh)^xsw)*16);
  lds_cptr xa1=(lds_cptr)lds+xr*128+(((4*kh+2+hh)^xsw)*16);
  const int blk=(lane>>4)&1, q4=(lane&15)>>2, p4=lane&3;
  lds_cptr wa=(lds_cptr)lds+8192+(32*kh+4*hh+q4)*128+((wn^((q4>>1)&1))*64)+blk*32+p4*8;
  f32x16 acc={};
  #pragma unroll 1
  for(int c=0;c<22;c++){
    if(c<20) asm volatile("s_waitcnt vmcnt(4) lgkmcnt(0)\n\ts_barrier":::"memory");
    else asm volatile("s_waitcnt vmcnt(0) lgkmcnt(0)\n\ts_barrier":::"memory");
    const int sb=(c&3)*16384;
    bf16x8 x0=lds_ld(xa0+sb), x1=lds_ld(xa1+sb);
    bf16x8 w0=cat8(lds_tr(wa+sb), lds_tr(wa+sb+1024));
    bf16x8 w1=cat8(lds_tr(wa+sb+2048), lds_tr(wa+sb+2048+1024));
    acc=MFMA32(w0,x0,acc); acc=MFMA32(w1,x1,acc);
    SB();
    if(c+3<22) issue(c+3);
  }
  DT(0);
  asm volatile("s_waitcnt lgkmcnt(0)\n\ts_barrier":::"memory");
  { const u16* ws_=wo+(long)prow*D+wc;
    #pragma unroll
    for(int c=0;c<8;c++) glds16(ws_+(long)c*64*D, lds0+c*8192+wid*1024); }
  const long ro=(long)(mt*64+xr)*D+nt*64+wn*32+4*hh;
  u32x2 xr4[4];
  #pragma unroll
  for(int q=0;q<4;q++) asm volatile("global_load_dwordx2 %0, %1, off offset:%2" : "=v"(xr4[q]) : "v"(x+ro), "n"(q*16) : "memory");
  asm volatile("s_waitcnt vmcnt(0)\n\ts_barrier" : "+v"(xr4[0]),"+v"(xr4[1]),"+v"(xr4[2]),"+v"(xr4[3]) :: "memory");
  bf16x8 wf[16];
  { lds_cptr wb=wa-8192;
    #pragma unroll
    for(int c=0;c<8;c++){
      wf[2*c]=cat8(lds_tr(wb+c*8192), lds_tr(wb+c*8192+1024));
      wf[2*c+1]=cat8(lds_tr(wb+c*8192+2048), lds_tr(wb+c*8192+2048+1024));
    } }
  asm volatile("s_waitcnt lgkmcnt(0)" : "+v"(wf[0]),"+v"(wf[1]),"+v"(wf[2]),"+v"(wf[3]),"+v"(wf[4]),"+v"(wf[5]),"+v"(wf[6]),"+v"(wf[7]),
      "+v"(wf[8]),"+v"(wf[9]),"+v"(wf[10]),"+v"(wf[11]),"+v"(wf[12]),"+v"(wf[13]),"+v"(wf[14]),"+v"(wf[15]) :: "memory");
  __builtin_amdgcn_s_setprio(0);
  if(tid==0){
    unsigned* cnt=ctl+(mt>>2)*32; unsigned spins=0;
    while(__hip_atomic_load(cnt,__ATOMIC_RELAXED,__HIP_MEMORY_SCOPE_AGENT)<32u){ __builtin_amdgcn_s_sleep(POLL_SLEEP); if(++spins>(1u<<24)) break; }
  }
  asm volatile("s_barrier":::"memory");
  DT(1);
  {
    const u16* os=ob+(long)(mt*64+prow)*D+sl*8;
    char* ow=lds+prow*128+((sl^((prow>>1)&7))*16);
    #pragma unroll
    for(int hb2=0;hb2<2;hb2++){
      u32x4 t0,t1,t2,t3;
      asm volatile("global_load_dwordx4 %0, %4, off offset:%5 sc1\n\tglobal_load_dwordx4 %1, %4, off offset:%6 sc1\n\tglobal_load_dwordx4 %2, %4, off offset:%7 sc1\n\tglobal_load_dwordx4 %3, %4, off offset:%8 sc1\n\ts_waitcnt vmcnt(0)"
        : "=&v"(t0),"=&v"(t1),"=&v"(t2),"=&v"(t3) : "v"(os), "n"(hb2*512), "n"(hb2*512+128), "n"(hb2*512+256), "n"(hb2*512+384) : "memory");
      *(u32x4*)(ow+(hb2*4+0)*8192)=t0; *(u32x4*)(ow+(hb2*4+1)*8192)=t1; *(u32x4*)(ow+(hb2*4+2)*8192)=t2; *(u32x4*)(ow+(hb2*4+3)*8192)=t3;
    } }
  asm volatile("s_waitcnt lgkmcnt(0)\n\ts_barrier":::"memory");
  DT(2);
  #pragma unroll
  for(int c=0;c<8;c++){
    bf16x8 x0=lds_ld(xa0+c*8192), x1=lds_ld(xa1+c*8192);
    acc=MFMA32(wf[2*c],x0,acc); acc=MFMA32(wf[2*c+1],x1,acc);
  }
  asm volatile("s_waitcnt lgkmcnt(0)\n\ts_barrier":::"memory");
  float* xch=(float*)lds+(wid&3)*1024+lane;
  if(kh==1){
    #pragma unroll
    for(int i=0;i<16;i++) xch[i*64]=acc[i];
  }
  __syncthreads();
  if(kh==1) return;
  #pragma unroll
  for(int q=0;q<4;q++){
    float v0=acc[4*q]+xch[(4*q)*64]+lo16f(xr4[q][0]), v1=acc[4*q+1]+xch[(4*q+1)*64]+hi16f(xr4[q][0]);
    float v2=acc[4*q+2]+xch[(4*q+2)*64]+lo16f(xr4[q][1]), v3=acc[4*q+3]+xch[(4*q+3)*64]+hi16f(xr4[q][1]);
    u32x2 w={pk2(v0,v1),pk2(v2,v3)};
    *(u32x2*)(out+ro+8*q)=w;
  }
}
__global__ __launch_bounds__(512,4) void k2_fused(const u16* __restrict__ Qs, const u16* __restrict__ Ks, const u16* __restrict__ Vt, u16* O,
     const u16* __restrict__ hb, const u16* __restrict__ x, const u16* __restrict__ wd, const u16* __restrict__ wo, u16* __restrict__ out, unsigned* ctl){
  extern __shared__ __attribute__((aligned(16))) char lds[];
  const int b=blockIdx.x;
  TL_BEGIN();
  int task;
  {
    int* sh=(int*)(lds+K2_LDS-16);
    if(threadIdx.x<64){
      const int ln=threadIdx.x;
      const unsigned tg=TGSEL;
      unsigned* claim=ctl+512;
      int t=(int)tg*256+(b&255);
      unsigned old=0;
      if(ln==0) old=__hip_atomic_fetch_add(claim+t,1u,__ATOMIC_RELAXED,__HIP_MEMORY_SCOPE_AGENT);
      old=(unsigned)__builtin_amdgcn_readfirstlane((int)old);
      if(old!=0u){
        t=-1;
        for(int blk=0; blk<8 && t<0; ){
          const int c=((blk<4)?(int)tg*256:256-(int)tg*256)+(blk&3)*64+ln;
          const unsigned v=__hip_atomic_load(claim+c,__ATOMIC_RELAXED,__HIP_MEMORY_SCOPE_AGENT);
          const unsigned long long fr=__ballot(v==0u);
          if(fr==0ull){ blk++; continue; }
          const int rot=b&63; const unsigned long long f2=(fr>>rot)|(rot?(fr<<(64-rot)):0ull);
          const int pick=(__builtin_ctzll(f2)+rot)&63;
          unsigned o2=1;
          if(ln==pick) o2=__hip_atomic_fetch_add(claim+c,1u,__ATOMIC_RELAXED,__HIP_MEMORY_SCOPE_AGENT);
          o2=(unsigned)__builtin_amdgcn_readlane((int)o2,pick);
          if(o2==0u) t=c-ln+pick;
        }
      }
      if(ln==0) *sh=t;
    }
    __syncthreads();
    task=__builtin_amdgcn_readfirstlane(*sh);
    __syncthreads();
  }
  if(task<0) return;
  if(task<256) attn_role(lds,Qs,Ks,Vt,O,task,ctl);
  else { const int u=task-256; down_role(lds,x,hb,wd,O,wo,out,ctl,(u&7)*4+((u>>3)&3),u>>5); }
  TL_END(1);
}

extern "C" void kernel_launch(void* const* d_in, const int* in_sizes, int n_in, void* d_out, int out_size, void* d_ws, size_t ws_size, hipStream_t stream){
  const u16* x=(const u16*)d_in[0]; const u16* wqkv=(const u16*)d_in[1]; const u16* wo=(const u16*)d_in[2];
  const u16* wg=(const u16*)d_in[4]; const u16* wu=(const u16*)d_in[5]; const u16* wd=(const u16*)d_in[6];
  char* ws=(char*)d_ws;
  unsigned* ctl=(unsigned*)ws; ws+=4096;
  u16* xn=(u16*)ws; ws+=S*D*2;
  u16* hb=(u16*)ws; ws+=(size_t)S*HP*2;
  u16* o=(u16*)ws; ws+=(size_t)S*D*2;
  u16* Qs=(u16*)ws; ws+=(size_t)S*D*2;
  u16* Ks=(u16*)ws; ws+=(size_t)S*D*2;
  u16* Vt=(u16*)ws; ws+=(size_t)S*D*2;
  float* yb=(float*)ws; ws+=(size_t)S*D*4;
  (void)hipFuncSetAttribute((const void*)k2_fused, hipFuncAttributeMaxDynamicSharedMemorySize, K2_LDS);
  (void)hipFuncSetAttribute((const void*)k1_qkv, hipFuncAttributeMaxDynamicSharedMemorySize, K1_LDS);
  k1_qkv<<<256,512,K1_LDS,stream>>>(x,wqkv,Qs,Ks,Vt,wg,wu,hb,ctl);
  k2_fused<<<512,512,K2_LDS,stream>>>(Qs,Ks,Vt,o,hb,x,wd,wo,(u16*)d_out,ctl);
}
```

```cpp
#define TGSEL ((__builtin_amdgcn_s_getreg((3<<11)|(16<<6)|4))&1u)
#define GROT ((((b>>3)&3)*4+((b&7)>>1))&15)
#define POLL_SLEEP 4
#define K1MAP 0
#define GU_DELAY 3
#define ABL 0
#define NBLK (256+352)
#define PACE 0
#define BARMODE 0
#define FIRSTW "0"
#include <hip/hip_runtime.h>
#include <hip/hip_bf16.h>
#include <stdint.h>

typedef unsigned short u16;
typedef __attribute__((ext_vector_type(8))) __bf16 bf16x8;
typedef __attribute__((ext_vector_type(2))) __bf16 bf16x2;
typedef __attribute__((ext_vector_type(2))) float f32x2;
typedef __attribute__((ext_vector_type(16))) float f32x16;
typedef __attribute__((ext_vector_type(4))) unsigned u32x4;
typedef __attribute__((ext_vector_type(2))) unsigned u32x2;
#define DI __device__ __forceinline__
#define MFMA32(a,b,c) __builtin_amdgcn_mfma_f32_32x32x16_bf16((a),(b),(c),0,0,0)

DI float bf2f(u16 b){ return __uint_as_float(((unsigned)b)<<16); }
DI u16 f2bf(float f){ unsigned u=__float_as_uint(f); u += 0x7fffu + ((u>>16)&1u); return (u16)(u>>16); }
DI float rbf(float f){ return bf2f(f2bf(f)); }
DI unsigned pk2(float lo, float hi){ f32x2 v={lo,hi}; bf16x2 b=__builtin_convertvector(v,bf16x2); return __builtin_bit_cast(unsigned,b); }

constexpr int S=2048, D=512, H=32, DH=16, F=1376;
constexpr float QSCALE = 0.25f*1.4426950408889634f;

DI void glds16(const void* g, unsigned lds_base) {
  unsigned sv; asm volatile("s_mov_b32 %0, m0\n\ts_mov_b32 m0, %2\n\ts_nop 0\n\tglobal_load_lds_dwordx4 %1, off\n\ts_mov_b32 m0, %0" : "=&s"(sv) : "v"(g), "s"(lds_base) : "memory"); }

typedef __attribute__((address_space(3))) const char* lds_cptr;
DI bf16x8 lds_ld(lds_cptr p){ return *(const __attribute__((address_space(3))) bf16x8*)p; }

#define SB() __builtin_amdgcn_sched_barrier(0)
typedef __attribute__((ext_vector_type(4))) float f32x4;
typedef __attribute__((ext_vector_type(4))) short s16x4;
typedef short v4i16_t __attribute__((ext_vector_type(4)));
#define MFMA16(a,b,c) __builtin_amdgcn_mfma_f32_16x16x32_bf16((a),(b),(c),0,0,0)
DI s16x4 lds_tr(lds_cptr p){ return __builtin_bit_cast(s16x4, __builtin_amdgcn_ds_read_tr16_b64_v4i16((__attribute__((address_space(3))) v4i16_t*)p)); }
DI bf16x8 cat8(s16x4 a, s16x4 b){ return __builtin_bit_cast(bf16x8, __builtin_shufflevector(a,b,0,1,2,3,4,5,6,7)); }
DI float dpp_add(float v, const int ctrl_sel){
  int t;
  if(ctrl_sel==0) t=__builtin_amdgcn_mov_dpp(__float_as_int(v),0xB1,0xF,0xF,true);
  else if(ctrl_sel==1) t=__builtin_amdgcn_mov_dpp(__float_as_int(v),0x4E,0xF,0xF,true);
  else t=__builtin_amdgcn_mov_dpp(__float_as_int(v),0x141,0xF,0xF,true);
  return v+__int_as_float(t);
}
DI float lo16f(unsigned u){ return __uint_as_float(u<<16); }
DI float hi16f(unsigned u){ return __uint_as_float(u&0xffff0000u); }
DI float dot2bf(unsigned a, unsigned b, float c){
  return __builtin_amdgcn_fdot2_f32_bf16(__builtin_bit_cast(bf16x2,a),__builtin_bit_cast(bf16x2,b),c,false); }

#define TL_BEGIN()
#define DT(i)
#define P1(i)
#define TL_END(k)
constexpr int HP=1408;
constexpr int K1_WST=24576, K1_NST=4, K1_X=K1_WST*K1_NST, K1_LDS=K1_X+8*8192;

template<int NT> DI void gu_stage(lds_cptr xa, lds_cptr wa, int xo0, int xo1, f32x16 (&ag)[2], f32x16 (&au)[2]){
  #pragma unroll
  for(int s=0;s<2;s++){
    bf16x8 gf=cat8(lds_tr(wa+s*6144), lds_tr(wa+s*6144+1536));
    bf16x8 uf=cat8(lds_tr(wa+12288+s*6144), lds_tr(wa+12288+s*6144+1536));
    #pragma unroll
    for(int t=0;t<NT;t++){
      bf16x8 xf=lds_ld(xa+t*4096+(s?xo1:xo0));
      ag[t]=MFMA32(gf,xf,ag[t]); au[t]=MFMA32(uf,xf,au[t]);
    }
  }
}
DI void gu_store(const f32x16& ag, const f32x16& au, u16* hp, bool zero){
  u32x4 o0,o1;
  #pragma unroll
  for(int i=0;i<8;i++){
    float hv[2];
    #pragma unroll
    for(int e=0;e<2;e++){ float g=ag[2*i+e]; float sg=g*__builtin_amdgcn_rcpf(1.0f+__builtin_amdgcn_exp2f(-1.4426950408889634f*g)); hv[e]=zero?0.f:sg*au[2*i+e]; }
    unsigned w=pk2(hv[0],hv[1]); if(i<4) o0[i]=w; else o1[i-4]=w;
  }
  *(u32x4*)hp=o0; *(u32x4*)(hp+16)=o1;
}

__global__ __launch_bounds__(512,2) void k1_qkv(const u16* __restrict__ x, const u16* __restrict__ wqkv,
     u16* __restrict__ Qs, u16* __restrict__ Ks, u16* __restrict__ Vt,
     const u16* __restrict__ wg, const u16* __restrict__ wu, u16* __restrict__ hb, unsigned* __restrict__ ctl){
  extern __shared__ __attribute__((aligned(16))) char lds[];
  const int tid=threadIdx.x, lane=tid&63; const int wid=__builtin_amdgcn_readfirstlane(tid>>6);
  const int b=blockIdx.x;
  const int rb=(b&7)*4+((b>>3)&3), hg=b>>5;
  const unsigned lds0=(unsigned)(uintptr_t)lds;
  const int rr=lane>>3, p=lane&7;
  const int lrow=wid*8+rr;
  TL_BEGIN();
  P1(0);
  const u16* xp=x+(long)(rb*64+lrow)*D+p*8;
  u32x4 xv[8];
  #pragma unroll
  for(int i=0;i<8;i++) asm volatile("global_load_dwordx4 %0, %1, off offset:%2" : "=v"(xv[i]) : "v"(xp), "n"(i*128) : "memory");
  const u16* wp=wqkv+(long)lrow*(3*D)+hg*64+(((p>>1)^(((lrow>>1)&1)|(((lrow>>3)&1)<<1)))*16)+(p&1)*8;
  asm volatile("s_barrier":::"memory");
  #pragma unroll
  for(int st=0;st<K1_NST;st++){
    #pragma unroll
    for(int ty=0;ty<3;ty++) glds16(wp+(long)st*64*(3*D)+ty*D, lds0+st*K1_WST+ty*8192+wid*1024);
  }
  const int ft0=hg*5+(hg<3?hg:3);
  int goff[3];
  #pragma unroll
  for(int i=0;i<3;i++){
    const int pc=wid*3+i; const int i16=(pc%12)*64+lane; const int k=i16/24, ch=i16%24;
    int col=ft0*32+(((ch>>2)^((k>>1)&1))*32)+(ch&3)*8; col=col>F-8?F-8:col;
    goff[i]=k*F+col;
  }
  const int rot=GROT;
  const u16* gbase=(wid<4)?wg:wu;
#define GU_ISS(g_) do{ _Pragma("unroll") for(int i=0;i<3;i++) glds16(gbase+(long)(((g_)+rot)&15)*32*F+goff[i], lds0+((g_)&3)*K1_WST+(wid*3+i)*1024); }while(0)
  asm volatile("s_waitcnt vmcnt(12)" : "+v"(xv[0]),"+v"(xv[1]),"+v"(xv[2]),"+v"(xv[3]),"+v"(xv[4]),"+v"(xv[5]),"+v"(xv[6]),"+v"(xv[7]) :: "memory");
  P1(1);
  float s1=0.f, s2=0.f;
  #pragma unroll
  for(int i=0;i<8;i++){
    #pragma unroll
    for(int j=0;j<4;j++){ s1=dot2bf(xv[i][j],0x3F803F80u,s1); s2=dot2bf(xv[i][j],xv[i][j],s2); }
  }
  s1=dpp_add(s1,0); s2=dpp_add(s2,0); s1=dpp_add(s1,1); s2=dpp_add(s2,1); s1=dpp_add(s1,2); s2=dpp_add(s2,2);
  const float mean=s1*(1.0f/D);
  float var=s2*(1.0f/D)-mean*mean;
  if(__builtin_expect(__any(!(mean*mean<=16.f*var)),0)){
    float q2=0.f;
    #pragma unroll
    for(int i=0;i<8;i++){
      #pragma unroll
      for(int j=0;j<4;j++){ float a=lo16f(xv[i][j])-mean, c=hi16f(xv[i][j])-mean; q2=fmaf(a,a,q2); q2=fmaf(c,c,q2); }
    }
    q2=dpp_add(q2,0); q2=dpp_add(q2,1); q2=dpp_add(q2,2);
    var=q2*(1.0f/D);
  }
  const float mb=rbf(mean);
  const float den=rbf(sqrtf(rbf(rbf(var)+1e-5f)));
  const float rinv=1.0f/den;
  char* xw=lds+K1_X+lrow*128+((p^((lrow>>1)&7))*16);
#define NRM1(i,j) do{ unsigned c_=pk2(lo16f(xv[i][j])-mb, hi16f(xv[i][j])-mb); xv[i][j]=pk2(lo16f(c_)*rinv, hi16f(c_)*rinv); }while(0)
#define NORM(i) do{ NRM1(i,0); NRM1(i,1); NRM1(i,2); NRM1(i,3); *(u32x4*)(xw+(i)*8192)=xv[i]; }while(0)
  NORM(0); NORM(1);
  const int rt2=wid&1, hd=wid>>1;
  const int g=lane>>4, r16=lane&15;
  const int xsw=(r16>>1)&7;
  lds_cptr xa0=(lds_cptr)lds+K1_X+(rt2*32+r16)*128+((g^xsw)*16);
  lds_cptr xa1=(lds_cptr)lds+K1_X+(rt2*32+r16)*128+(((4+g)^xsw)*16);
  const int q4=(lane&15)>>2, p4=lane&3;
  const int fk=((q4>>1)&1)|((g&1)<<1);
  lds_cptr wa=(lds_cptr)lds+(8*g+q4)*128+((hd^fk)*32)+p4*8;
  f32x4 aq[2]={}, ak[2]={}, av[2]={};
  bf16x8 fx[2][2][2], fw[2][2][3];
#define TRW(set,s,ty,c) fw[set][s][ty]=cat8(lds_tr(wa+((c)%K1_NST)*K1_WST+(ty)*8192+(s)*4096), lds_tr(wa+((c)%K1_NST)*K1_WST+(ty)*8192+(s)*4096+512))
  asm volatile("s_waitcnt vmcnt(9) lgkmcnt(0)\n\ts_barrier":::"memory");
  P1(2);
  fx[0][0][0]=lds_ld(xa0); fx[0][0][1]=lds_ld(xa0+2048); fx[0][1][0]=lds_ld(xa1); fx[0][1][1]=lds_ld(xa1+2048);
  TRW(0,0,0,0); TRW(0,0,1,0); TRW(0,0,2,0); TRW(0,1,0,0); TRW(0,1,1,0); TRW(0,1,2,0);
#define K1_ISS1(c,i) do{ if((c)<4) glds16(wp+(long)((c)+4)*64*(3*D)+(i)*D, lds0+(c)*K1_WST+(i)*8192+wid*1024); \
                        else if((c)<7) glds16(gbase+(long)(((c)-4+rot)&15)*32*F+goff[i], lds0+((c)-4)*K1_WST+(wid*3+(i))*1024); }while(0)
  #pragma unroll
  for(int c=0;c<8;c++){
    const int cs=c&1, ns=cs^1; const int xb=(c+1)*8192; const int ni=(c+2)&7;
    if(c<7) asm volatile("s_waitcnt vmcnt(6) lgkmcnt(0)\n\ts_barrier":::"memory");
    aq[0]=MFMA16(fw[cs][0][0],fx[cs][0][0],aq[0]); if(c<7){ fx[ns][0][0]=lds_ld(xa0+xb); fx[ns][0][1]=lds_ld(xa0+xb+2048); } SB();
    aq[1]=MFMA16(fw[cs][0][0],fx[cs][0][1],aq[1]); if(c<7){ TRW(ns,0,0,c+1); } SB();
    ak[0]=MFMA16(fw[cs][0][1],fx[cs][0][0],ak[0]); if(c<6) NRM1(ni,0); SB();
    ak[1]=MFMA16(fw[cs][0][1],fx[cs][0][1],ak[1]); if(c<7){ TRW(ns,0,1,c+1); } SB();
    av[0]=MFMA16(fx[cs][0][0],fw[cs][0][2],av[0]); if(c<7){ TRW(ns,0,2,c+1); } K1_ISS1(c,0); SB();
    av[1]=MFMA16(fx[cs][0][1],fw[cs][0][2],av[1]); if(c<6) NRM1(ni,1); SB();
    aq[0]=MFMA16(fw[cs][1][0],fx[cs][1][0],aq[0]); if(c<7){ fx[ns][1][0]=lds_ld(xa1+xb); fx[ns][1][1]=lds_ld(xa1+xb+2048); } SB();
    aq[1]=MFMA16(fw[cs][1][0],fx[cs][1][1],aq[1]); if(c<7){ TRW(ns,1,0,c+1); } K1_ISS1(c,1); SB();
    ak[0]=MFMA16(fw[cs][1][1],fx[cs][1][0],ak[0]); if(c<6) NRM1(ni,2); SB();
    ak[1]=MFMA16(fw[cs][1][1],fx[cs][1][1],ak[1]); if(c<7){ TRW(ns,1,1,c+1); } SB();
    av[0]=MFMA16(fx[cs][1][0],fw[cs][1][2],av[0]); if(c<7){ TRW(ns,1,2,c+1); } K1_ISS1(c,2); SB();
    av[1]=MFMA16(fx[cs][1][1],fw[cs][1][2],av[1]); if(c<6){ NRM1(ni,3); *(u32x4*)(xw+ni*8192)=xv[ni]; } SB();
  }
  P1(3);
  {
    const int h=hg*4+hd; const int T=rb*2+rt2;
    #pragma unroll
    for(int tt=0;tt<2;tt++){
      const long qo=(long)h*S*DH+T*512+(g>>1)*256+(tt*16+r16)*8+(g&1)*4;
      u32x2 wq={pk2(aq[tt][0]*QSCALE,aq[tt][1]*QSCALE),pk2(aq[tt][2]*QSCALE,aq[tt][3]*QSCALE)};
      u32x2 wk={pk2(ak[tt][0],ak[tt][1]),pk2(ak[tt][2],ak[tt][3])};
      const long vo=(long)h*S*DH+(2*T+tt)*256+(g&1)*128+r16*8+(g>>1)*4;
      u32x2 wv={pk2(av[tt][0],av[tt][1]),pk2(av[tt][2],av[tt][3])};
      asm volatile("global_store_dwordx2 %0, %1, off\n\tglobal_store_dwordx2 %2, %3, off\n\tglobal_store_dwordx2 %4, %5, off"
         :: "v"(Qs+qo),"v"(wq),"v"(Ks+qo),"v"(wk),"v"(Vt+vo),"v"(wv) : "memory");
    }
  }
  {
    const int r=lane&31, hh=lane>>5;
    const int ftl = wid<4 ? wid : 4+((wid-4)>>1);
    const int tt0 = wid<4 ? 0 : ((wid-4)&1);
    const int xsw2=(r>>1)&7;
    lds_cptr xg=(lds_cptr)lds+K1_X+(tt0*32+r)*128;
    const int blk=(lane>>4)&1;
    lds_cptr wg_=(lds_cptr)lds+(8*hh+q4)*384+((ftl^((q4>>1)&1))*64)+blk*32+p4*8;
    f32x16 ag[2]={}, au[2]={};
    bf16x8 gf[2][2], uf[2][2], xf[2][2][2];
    const bool two=(wid<4);
#define GRD(gs_,s) do{ const int st_=(gs_)&1; lds_cptr w_=wg_+((gs_)&3)*K1_WST+(s)*6144; \
      gf[st_][s]=cat8(lds_tr(w_), lds_tr(w_+1536)); uf[st_][s]=cat8(lds_tr(w_+12288), lds_tr(w_+12288+1536)); \
      const int gr_=((gs_)+rot)&15; lds_cptr x_=xg+(gr_>>1)*8192+(((4*(gr_&1)+2*(s)+hh)^xsw2)*16); xf[st_][s][0]=lds_ld(x_); if(two) xf[st_][s][1]=lds_ld(x_+4096); }while(0)
#define GMM(gs_,s) do{ const int st_=(gs_)&1; ag[0]=MFMA32(gf[st_][s],xf[st_][s][0],ag[0]); au[0]=MFMA32(uf[st_][s],xf[st_][s][0],au[0]); \
      if(two){ ag[1]=MFMA32(gf[st_][s],xf[st_][s][1],ag[1]); au[1]=MFMA32(uf[st_][s],xf[st_][s][1],au[1]); } }while(0)
#define GU_ISS1(g_,i) glds16(gbase+(long)(((g_)+rot)&15)*32*F+goff[i], lds0+((g_)&3)*K1_WST+(wid*3+(i))*1024)
    asm volatile("s_waitcnt vmcnt(12) lgkmcnt(0)\n\ts_barrier":::"memory");
    P1(4);
    GRD(0,0); GRD(0,1);
    GU_ISS(3);
#define GWAIT(gs) do{ \
      if((gs)<2) asm volatile("s_waitcnt vmcnt(12) lgkmcnt(0)\n\ts_barrier":::"memory"); \
      else if((gs)<13) asm volatile("s_waitcnt vmcnt(6) lgkmcnt(0)\n\ts_barrier":::"memory"); \
      else if((gs)==13) asm volatile("s_waitcnt vmcnt(3) lgkmcnt(0)\n\ts_barrier":::"memory"); \
      else if((gs)==14) asm volatile("s_waitcnt vmcnt(0) lgkmcnt(0)\n\ts_barrier":::"memory"); }while(0)
#define RG(gn,s) do{ if((gn)<16){ lds_cptr w_=wg_+((gn)&3)*K1_WST+(s)*6144; gf[(gn)&1][s]=cat8(lds_tr(w_), lds_tr(w_+1536)); } }while(0)
#define RU(gn,s) do{ if((gn)<16){ lds_cptr w_=wg_+((gn)&3)*K1_WST+(s)*6144+12288; uf[(gn)&1][s]=cat8(lds_tr(w_), lds_tr(w_+1536)); } }while(0)
#define RX(gn,s,t) do{ if((gn)<16){ const int gr_=((gn)+rot)&15; xf[(gn)&1][s][t]=lds_ld(xg+(gr_>>1)*8192+(((4*(gr_&1)+2*(s)+hh)^xsw2)*16)+(t)*4096); } }while(0)
#define MG(gs,s,t) ag[t]=MFMA32(gf[(gs)&1][s],xf[(gs)&1][s][t],ag[t])
#define MU(gs,s,t) au[t]=MFMA32(uf[(gs)&1][s],xf[(gs)&1][s][t],au[t])
#define GI(gs,i) do{ if((gs)+4<16) GU_ISS1((gs)+4,i); }while(0)
    if(two){
      #pragma unroll
      for(int gs=0;gs<16;gs++){
        GWAIT(gs);
        MG(gs,0,0); RG(gs+1,0); SB();
        MU(gs,0,0); RU(gs+1,0); SB();
        MG(gs,0,1); RX(gs+1,0,0); RX(gs+1,0,1); SB();
        MU(gs,0,1); RG(gs+1,1); SB();
        MG(gs,1,0); RU(gs+1,1); SB();
        MU(gs,1,0); RX(gs+1,1,0); RX(gs+1,1,1); SB();
        MG(gs,1,1); GI(gs,0); GI(gs,1); SB();
        MU(gs,1,1); GI(gs,2); SB();
      }
    } else if((ftl<5)||(hg<3)) {
      #pragma unroll
      for(int gs=0;gs<16;gs++){
        GWAIT(gs);
        GI(gs,0); SB();
        MG(gs,0,0); RG(gs+1,0); RU(gs+1,0); SB();
        MU(gs,0,0); RX(gs+1,0,0); RG(gs+1,1); SB();
        MG(gs,1,0); RU(gs+1,1); RX(gs+1,1,0); SB();
        MU(gs,1,0); GI(gs,1); GI(gs,2); SB();
      }
    } else {
      #pragma unroll
      for(int gs=0;gs<16;gs++){ GWAIT(gs); GI(gs,0); GI(gs,1); GI(gs,2); }
    }
    P1(5);
    const int ft=ft0+ftl;
    const bool own=(ftl<5)||(hg<3)||(hg==7);
    if(own){
      u16* hp=hb+(long)(rb*64+tt0*32+r)*HP+ft*32+hh*8;
      gu_store(ag[0],au[0],hp,ft>=43);
      if(wid<4) gu_store(ag[1],au[1],hp+(long)32*HP,false);
    }
  }
  if(b==0){ ctl[tid]=0u; ctl[512+tid]=0u; }
  TL_END(0);
}

#define GU_PRIO 2
constexpr int A2_K=0, A2_V=32768, A2_C=65536, K2_LDS=65536+4096+16;
#define EX(x) __builtin_amdgcn_exp2f(x)
DI void pv_step(const f32x16& sc, bf16x8 v0, bf16x8 v1, f32x16& o){
  u32x4 p0, p1;
  #pragma unroll
  for(int i=0;i<4;i++){ p0[i]=pk2(EX(sc[2*i]),EX(sc[2*i+1])); }
  #pragma unroll
  for(int i=0;i<4;i++){ p1[i]=pk2(EX(sc[8+2*i]),EX(sc[8+2*i+1])); }
  o=MFMA32(v0,__builtin_bit_cast(bf16x8,p0),o);
  o=MFMA32(v1,__builtin_bit_cast(bf16x8,p1),o);
}
DI void attn_role(char* lds, const u16* __restrict__ Qs, const u16* __restrict__ Ks, const u16* __restrict__ Vt, u16* O, int b, unsigned* ctl){
  const int tid=threadIdx.x, lane=tid&63; const int wid=__builtin_amdgcn_readfirstlane(tid>>6);
  const int h=(b&7)*4+((b>>3)>>3), qb=(b>>3)&7;
  const int r=lane&31, hh=lane>>5;
  const unsigned lds0=(unsigned)(uintptr_t)lds;
  const u16* qp=Qs+(long)h*S*DH+(qb*8+wid)*512+lane*8;
  bf16x8 qf;
  asm volatile("global_load_dwordx4 %0, %1, off" : "=v"(qf) : "v"(qp) : "memory");
  const u16* kg=Ks+(long)h*S*DH+wid*512+lane*8;
  const u16* vg=Vt+(long)h*S*DH+wid*512+lane*8;
#define A_ISSUE(c) do{ glds16(kg+(c)*4096, lds0+A2_K+((c)&3)*8192+wid*1024); glds16(vg+(c)*4096, lds0+A2_V+((c)&3)*8192+wid*1024); }while(0)
  A_ISSUE(0); A_ISSUE(1); A_ISSUE(2); A_ISSUE(3);
  { u32x2 v; v[0]=((tid&31)<2)?0x3F803F80u:0u; v[1]=v[0]; *(u32x2*)(lds+A2_C+tid*8)=v; }
  lds_cptr kp=(lds_cptr)lds+A2_K+lane*16;
  const bool cl=(r>=16);
  lds_cptr vp=(lds_cptr)lds+(cl ? A2_C : A2_V)+hh*256+(r&15)*16;
  const int vinc= cl?0:1024; const int vwrap= cl?0:(1024-32768);
  asm volatile("s_waitcnt vmcnt(6) lgkmcnt(0)\n\ts_barrier": "+v"(qf) ::"memory");
  f32x16 negm; f32x16 sA, sB;
  bf16x8 kA, kB, vA0, vA1, vB0, vB1;
  {
    f32x16 z={}; bf16x8 k0=lds_ld(kp);
    kA=lds_ld(kp+1024); vA0=lds_ld(vp); vA1=lds_ld(vp+512); vp+=vinc;
    f32x16 s0=MFMA32(k0,qf,z);
    float m=s0[0];
    #pragma unroll
    for(int i=1;i<16;i++) m=fmaxf(m,s0[i]);
    #pragma unroll
    for(int t=1;t<4;t++){
      f32x16 st=MFMA32(t==1?kA:lds_ld(kp+t*1024),qf,z);
      #pragma unroll
      for(int i=0;i<16;i++) m=fmaxf(m,st[i]);
    }
    auto rr=__builtin_amdgcn_permlane32_swap(__float_as_uint(m),__float_as_uint(m),false,false);
    m=fmaxf(__uint_as_float(rr[0]),__uint_as_float(rr[1]));
    float nm=-(m+80.f);
    #pragma unroll
    for(int i=0;i<16;i++){ negm[i]=nm; sA[i]=s0[i]+nm; }
  }
  f32x16 o={};
#define STEP(sC,sN,kU,kL,vU0,vU1,vL0,vL1,t) do{ \
    u32x4 p0,p1; \
    kL=lds_ld(kp+(((t)+2)&31)*1024); vL0=lds_ld(vp); vL1=lds_ld(vp+512); vp+=((((t)+1)&31)==31)?vwrap:vinc; \
    float e0=EX(sC[0]),e1=EX(sC[1]),e2=EX(sC[2]),e3=EX(sC[3]); SB(); \
    sN=MFMA32(kU,qf,negm); SB(); \
    float e4=EX(sC[4]),e5=EX(sC[5]),e6=EX(sC[6]),e7=EX(sC[7]); \
    p0[0]=pk2(e0,e1); p0[1]=pk2(e2,e3); p0[2]=pk2(e4,e5); p0[3]=pk2(e6,e7); SB(); \
    float e8=EX(sC[8]),e9=EX(sC[9]); SB(); \
    o=MFMA32(vU0,__builtin_bit_cast(bf16x8,p0),o); SB(); \
    float e10=EX(sC[10]),e11=EX(sC[11]),e12=EX(sC[12]),e13=EX(sC[13]),e14=EX(sC[14]),e15=EX(sC[15]); \
    p1[0]=pk2(e8,e9); p1[1]=pk2(e10,e11); p1[2]=pk2(e12,e13); p1[3]=pk2(e14,e15); SB(); \
    o=MFMA32(vU1,__builtin_bit_cast(bf16x8,p1),o); SB(); \
  }while(0)
#define ITER(t) do{ STEP(sA,sB,kA,kB,vA0,vA1,vB0,vB1,(t)); STEP(sB,sA,kB,kA,vB0,vB1,vA0,vA1,(t)+1); }while(0)
  #pragma unroll
  for(int j=0;j<8;j++){
    ITER(8*j); ITER(8*j+2); ITER(8*j+4);
    if(j<7){
      if(j==0) asm volatile("s_waitcnt vmcnt(4) lgkmcnt(0)\n\ts_barrier":::"memory");
      else if(j<6) asm volatile("s_waitcnt vmcnt(2) lgkmcnt(0)\n\ts_barrier":::"memory");
      else asm volatile("s_waitcnt vmcnt(0) lgkmcnt(0)\n\ts_barrier":::"memory");
      if(j>=1 && j+3<8) A_ISSUE(j+3);
    }
    ITER(8*j+6);
  }
  float l=o[8];
  { auto rr=__builtin_amdgcn_permlane32_swap(__float_as_uint(l),__float_as_uint(l),false,false); l=__uint_as_float(rr[0]); }
  bool bad = !(l<1e36f);
  #pragma unroll
  for(int i=0;i<8;i++) bad = bad || !(fabsf(o[i])<3e38f);
  if(__builtin_expect(__any(bad),0)){
    const u16* kgl=Ks+(long)h*S*DH+lane*8;
    const u16* vgl=Vt+(long)h*S*DH+hh*128+(r&15)*8;
    float m=-INFINITY; f32x16 z={};
    #pragma unroll
    for(int i=0;i<16;i++) o[i]=0.f;
    for(int t=0;t<64;t++){
      bf16x8 kf=*(const bf16x8*)(kgl+t*512);
      bf16x8 v0=*(const bf16x8*)(vgl+t*512), v1=*(const bf16x8*)(vgl+t*512+256);
      if(cl){ u32x4 one={0x3F803F80u,0x3F803F80u,0x3F803F80u,0x3F803F80u}; v0=__builtin_bit_cast(bf16x8,one); v1=v0; }
      f32x16 s0=MFMA32(kf,qf,z);
      float tm=s0[0];
      #pragma unroll
      for(int i=1;i<16;i++) tm=fmaxf(tm,s0[i]);
      auto rr=__builtin_amdgcn_permlane32_swap(__float_as_uint(tm),__float_as_uint(tm),false,false);
      tm=fmaxf(__uint_as_float(rr[0]),__uint_as_float(rr[1]));
      float mn=fmaxf(m,tm);
      float alpha=EX(m-mn); m=mn;
      #pragma unroll
      for(int i=0;i<16;i++){ o[i]*=alpha; s0[i]-=mn; }
      pv_step(s0,v0,v1,o);
    }
    l=o[8];
    auto rr=__builtin_amdgcn_permlane32_swap(__float_as_uint(l),__float_as_uint(l),false,false); l=__uint_as_float(rr[0]);
  }
  float rl=1.0f/l;
  u32x4 w={pk2(o[0]*rl,o[1]*rl),pk2(o[2]*rl,o[3]*rl),pk2(o[4]*rl,o[5]*rl),pk2(o[6]*rl,o[7]*rl)};
  asm volatile("global_store_dwordx4 %0, %1, off sc1\n\ts_waitcnt vmcnt(0)\n\ts_barrier" :: "v"(O+(long)((qb*8+wid)*32+r)*D+h*DH+hh*8), "v"(w) : "memory");
  if(tid==0) __hip_atomic_fetch_add(ctl+qb*32,1u,__ATOMIC_RELAXED,__HIP_MEMORY_SCOPE_AGENT);
}
DI void down_role(char* lds, const u16* __restrict__ x, const u16* __restrict__ hb, const u16* __restrict__ wd, const u16* ob, const u16* __restrict__ wo, u16* __restrict__ out, unsigned* ctl, int mt, int nt){
  const int tid=threadIdx.x, lane=tid&63; const int wid=__builtin_amdgcn_readfirstlane(tid>>6);
  const unsigned lds0=(unsigned)(uintptr_t)lds;
  __builtin_amdgcn_s_setprio(GU_PRIO);
  for(int i=0;i<GU_DELAY;i++) __builtin_amdgcn_s_sleep(16);
  const int rr=lane>>3, sl=lane&7;
  const int prow=wid*8+rr;
  const u16* hs=hb+(long)(mt*64+prow)*HP+((sl^((prow>>1)&7))*8);
  const int wc=nt*64+(((sl>>2)^((prow>>1)&1))*32)+(sl&3)*8;
  auto issue=[&](int c){ const unsigned b_=lds0+(c&3)*16384+wid*1024; int k=c*64+prow; k=k>F-1?F-1:k;
    glds16(hs+c*64,b_); glds16(wd+(long)k*D+wc,b_+8192); };
  issue(0); issue(1); issue(2);
  const int wm=wid&1, wn=(wid>>1)&1, kh=wid>>2; const int r=lane&31, hh=lane>>5;
  const int xr=wm*32+r; const int xsw=(xr>>1)&7;
  lds_cptr xa0=(lds_cptr)lds+xr*128+(((4*kh+hh)^xsw)*16);
  lds_cptr xa1=(lds_cptr)lds+xr*128+(((4*kh+2+hh)^xsw)*16);
  const int blk=(lane>>4)&1, q4=(lane&15)>>2, p4=lane&3;
  lds_cptr wa=(lds_cptr)lds+8192+(32*kh+4*hh+q4)*128+((wn^((q4>>1)&1))*64)+blk*32+p4*8;
  f32x16 acc={};
  #pragma unroll 1
  for(int c=0;c<22;c++){
    if(c<20) asm volatile("s_waitcnt vmcnt(4) lgkmcnt(0)\n\ts_barrier":::"memory");
    else asm volatile("s_waitcnt vmcnt(0) lgkmcnt(0)\n\ts_barrier":::"memory");
    const int sb=(c&3)*16384;
    bf16x8 x0=lds_ld(xa0+sb), x1=lds_ld(xa1+sb);
    bf16x8 w0=cat8(lds_tr(wa+sb), lds_tr(wa+sb+1024));
    bf16x8 w1=cat8(lds_tr(wa+sb+2048), lds_tr(wa+sb+2048+1024));
    acc=MFMA32(w0,x0,acc); acc=MFMA32(w1,x1,acc);
    SB();
    if(c+3<22) issue(c+3);
  }
  DT(0);
  asm volatile("s_waitcnt lgkmcnt(0)\n\ts_barrier":::"memory");
  { const u16* ws_=wo+(long)prow*D+wc;
    #pragma unroll
    for(int c=0;c<8;c++) glds16(ws_+(long)c*64*D, lds0+c*8192+wid*1024); }
  const long ro=(long)(mt*64+xr)*D+nt*64+wn*32+4*hh;
  asm volatile("s_waitcnt vmcnt(0)\n\ts_barrier":::"memory");
  bf16x8 wf[16];
  { lds_cptr wb=wa-8192;
    #pragma unroll
    for(int c=0;c<8;c++){
      wf[2*c]=cat8(lds_tr(wb+c*8192), lds_tr(wb+c*8192+1024));
      wf[2*c+1]=cat8(lds_tr(wb+c*8192+2048), lds_tr(wb+c*8192+2048+1024));
    } }
  asm volatile("s_waitcnt lgkmcnt(0)" : "+v"(wf[0]),"+v"(wf[1]),"+v"(wf[2]),"+v"(wf[3]),"+v"(wf[4]),"+v"(wf[5]),"+v"(wf[6]),"+v"(wf[7]),
      "+v"(wf[8]),"+v"(wf[9]),"+v"(wf[10]),"+v"(wf[11]),"+v"(wf[12]),"+v"(wf[13]),"+v"(wf[14]),"+v"(wf[15]) :: "memory");
  __builtin_amdgcn_s_setprio(0);
  if(tid==0){
    unsigned* cnt=ctl+(mt>>2)*32; unsigned spins=0;
    while(__hip_atomic_load(cnt,__ATOMIC_RELAXED,__HIP_MEMORY_SCOPE_AGENT)<32u){ __builtin_amdgcn_s_sleep(POLL_SLEEP); if(++spins>(1u<<21)) break; }
  }
  asm volatile("s_barrier":::"memory");
  DT(1);
  {
    const u16* os=ob+(long)(mt*64+prow)*D+sl*8;
    char* ow=lds+prow*128+((sl^((prow>>1)&7))*16);
    u32x4 t0,t1,t2,t3,t4,t5,t6,t7;
    asm volatile("global_load_dwordx4 %0, %8, off sc1\n\tglobal_load_dwordx4 %1, %8, off offset:128 sc1\n\tglobal_load_dwordx4 %2, %8, off offset:256 sc1\n\tglobal_load_dwordx4 %3, %8, off offset:384 sc1\n\t"
                 "global_load_dwordx4 %4, %8, off offset:512 sc1\n\tglobal_load_dwordx4 %5, %8, off offset:640 sc1\n\tglobal_load_dwordx4 %6, %8, off offset:768 sc1\n\tglobal_load_dwordx4 %7, %8, off offset:896 sc1\n\ts_waitcnt vmcnt(0)"
      : "=&v"(t0),"=&v"(t1),"=&v"(t2),"=&v"(t3),"=&v"(t4),"=&v"(t5),"=&v"(t6),"=&v"(t7) : "v"(os) : "memory");
    *(u32x4*)(ow)=t0; *(u32x4*)(ow+8192)=t1; *(u32x4*)(ow+2*8192)=t2; *(u32x4*)(ow+3*8192)=t3;
    *(u32x4*)(ow+4*8192)=t4; *(u32x4*)(ow+5*8192)=t5; *(u32x4*)(ow+6*8192)=t6; *(u32x4*)(ow+7*8192)=t7;
  }
  u32x2 xr4[4];
  #pragma unroll
  for(int q=0;q<4;q++) xr4[q]=*(const u32x2*)(x+ro+8*q);
  asm volatile("s_waitcnt lgkmcnt(0)\n\ts_barrier":::"memory");
  DT(2);
  #pragma unroll
  for(int c=0;c<8;c++){
    bf16x8 x0=lds_ld(xa0+c*8192), x1=lds_ld(xa1+c*8192);
    acc=MFMA32(wf[2*c],x0,acc); acc=MFMA32(wf[2*c+1],x1,acc);
  }
  asm volatile("s_waitcnt lgkmcnt(0)\n\ts_barrier":::"memory");
  float* xch=(float*)lds+(wid&3)*1024+lane;
  if(kh==1){
    #pragma unroll
    for(int i=0;i<16;i++) xch[i*64]=acc[i];
  }
  __syncthreads();
  if(kh==1) return;
  #pragma unroll
  for(int q=0;q<4;q++){
    float v0=acc[4*q]+xch[(4*q)*64]+lo16f(xr4[q][0]), v1=acc[4*q+1]+xch[(4*q+1)*64]+hi16f(xr4[q][0]);
    float v2=acc[4*q+2]+xch[(4*q+2)*64]+lo16f(xr4[q][1]), v3=acc[4*q+3]+xch[(4*q+3)*64]+hi16f(xr4[q][1]);
    u32x2 w={pk2(v0,v1),pk2(v2,v3)};
    *(u32x2*)(out+ro+8*q)=w;
  }
}
__global__ __launch_bounds__(512,4) void k2_fused(const u16* __restrict__ Qs, const u16* __restrict__ Ks, const u16* __restrict__ Vt, u16* O,
     const u16* __restrict__ hb, const u16* __restrict__ x, const u16* __restrict__ wd, const u16* __restrict__ wo, u16* __restrict__ out, unsigned* ctl){
  extern __shared__ __attribute__((aligned(16))) char lds[];
  const int b=blockIdx.x;
  TL_BEGIN();
  int task;
  {
    int* sh=(int*)(lds+K2_LDS-16);
    if(threadIdx.x<64){
      const int ln=threadIdx.x;
      const unsigned tg=TGSEL;
      unsigned* claim=ctl+512;
      int t=(int)tg*256+(b&255);
      unsigned old=0;
      if(ln==0) old=__hip_atomic_fetch_add(claim+t,1u,__ATOMIC_RELAXED,__HIP_MEMORY_SCOPE_AGENT);
      old=(unsigned)__builtin_amdgcn_readfirstlane((int)old);
      if(old!=0u){
        t=-1;
        for(int blk=0; blk<8 && t<0; ){
          const int c=((blk<4)?(int)tg*256:256-(int)tg*256)+(blk&3)*64+ln;
          const unsigned v=__hip_atomic_load(claim+c,__ATOMIC_RELAXED,__HIP_MEMORY_SCOPE_AGENT);
          const unsigned long long fr=__ballot(v==0u);
          if(fr==0ull){ blk++; continue; }
          const int rot=b&63; const unsigned long long f2=(fr>>rot)|(rot?(fr<<(64-rot)):0ull);
          const int pick=(__builtin_ctzll(f2)+rot)&63;
          unsigned o2=1;
          if(ln==pick) o2=__hip_atomic_fetch_add(claim+c,1u,__ATOMIC_RELAXED,__HIP_MEMORY_SCOPE_AGENT);
          o2=(unsigned)__builtin_amdgcn_readlane((int)o2,pick);
          if(o2==0u) t=c-ln+pick;
        }
      }
      if(ln==0) *sh=t;
    }
    __syncthreads();
    task=__builtin_amdgcn_readfirstlane(*sh);
    __syncthreads();
  }
  if(task<0) return;
  if(task<256) attn_role(lds,Qs,Ks,Vt,O,task,ctl);
  else { const int u=task-256; down_role(lds,x,hb,wd,O,wo,out,ctl,(u&7)*4+((u>>3)&3),u>>5); }
  TL_END(1);
}

extern "C" void kernel_launch(void* const* d_in, const int* in_sizes, int n_in, void* d_out, int out_size, void* d_ws, size_t ws_size, hipStream_t stream){
  const u16* x=(const u16*)d_in[0]; const u16* wqkv=(const u16*)d_in[1]; const u16* wo=(const u16*)d_in[2];
  const u16* wg=(const u16*)d_in[4]; const u16* wu=(const u16*)d_in[5]; const u16* wd=(const u16*)d_in[6];
  char* ws=(char*)d_ws;
  unsigned* ctl=(unsigned*)ws; ws+=4096;
  u16* xn=(u16*)ws; ws+=S*D*2;
  u16* hb=(u16*)ws; ws+=(size_t)S*HP*2;
  u16* o=(u16*)ws; ws+=(size_t)S*D*2;
  u16* Qs=(u16*)ws; ws+=(size_t)S*D*2;
  u16* Ks=(u16*)ws; ws+=(size_t)S*D*2;
  u16* Vt=(u16*)ws; ws+=(size_t)S*D*2;
  float* yb=(float*)ws; ws+=(size_t)S*D*4;
  (void)hipFuncSetAttribute((const void*)k2_fused, hipFuncAttributeMaxDynamicSharedMemorySize, K2_LDS);
  (void)hipFuncSetAttribute((const void*)k1_qkv, hipFuncAttributeMaxDynamicSharedMemorySize, K1_LDS);
  k1_qkv<<<256,512,K1_LDS,stream>>>(x,wqkv,Qs,Ks,Vt,wg,wu,hb,ctl);
  k2_fused<<<512,512,K2_LDS,stream>>>(Qs,Ks,Vt,o,hb,x,wd,wo,(u16*)d_out,ctl);
}
```

```cpp
#define TGSEL ((__builtin_amdgcn_s_getreg((3<<11)|(16<<6)|4))&1u)
#define GROT ((((b>>3)&3)*4+((b&7)>>1))&15)
#define POLL_SLEEP 4
#define GU_DELAY 3
#include <hip/hip_runtime.h>
#include <hip/hip_bf16.h>
#include <stdint.h>

typedef unsigned short u16;
typedef __attribute__((ext_vector_type(8))) __bf16 bf16x8;
typedef __attribute__((ext_vector_type(2))) __bf16 bf16x2;
typedef __attribute__((ext_vector_type(2))) float f32x2;
typedef __attribute__((ext_vector_type(16))) float f32x16;
typedef __attribute__((ext_vector_type(4))) unsigned u32x4;
typedef __attribute__((ext_vector_type(2))) unsigned u32x2;
#define DI __device__ __forceinline__
#define MFMA32(a,b,c) __builtin_amdgcn_mfma_f32_32x32x16_bf16((a),(b),(c),0,0,0)

DI float bf2f(u16 b){ return __uint_as_float(((unsigned)b)<<16); }
DI u16 f2bf(float f){ unsigned u=__float_as_uint(f); u += 0x7fffu + ((u>>16)&1u); return (u16)(u>>16); }
DI float rbf(float f){ return bf2f(f2bf(f)); }
DI unsigned pk2(float lo, float hi){ f32x2 v={lo,hi}; bf16x2 b=__builtin_convertvector(v,bf16x2); return __builtin_bit_cast(unsigned,b); }

constexpr int S=2048, D=512, H=32, DH=16, F=1376;
constexpr float QSCALE = 0.25f*1.4426950408889634f;

DI void glds16(const void* g, unsigned lds_base) {
  unsigned sv; asm volatile("s_mov_b32 %0, m0\n\ts_mov_b32 m0, %2\n\ts_nop 0\n\tglobal_load_lds_dwordx4 %1, off\n\ts_mov_b32 m0, %0" : "=&s"(sv) : "v"(g), "s"(lds_base) : "memory"); }

typedef __attribute__((address_space(3))) const char* lds_cptr;
DI bf16x8 lds_ld(lds_cptr p){ return *(const __attribute__((address_space(3))) bf16x8*)p; }

#define SB() __builtin_amdgcn_sched_barrier(0)
typedef __attribute__((ext_vector_type(4))) float f32x4;
typedef __attribute__((ext_vector_type(4))) short s16x4;
typedef short v4i16_t __attribute__((ext_vector_type(4)));
#define MFMA16(a,b,c) __builtin_amdgcn_mfma_f32_16x16x32_bf16((a),(b),(c),0,0,0)
DI s16x4 lds_tr(lds_cptr p){ return __builtin_bit_cast(s16x4, __builtin_amdgcn_ds_read_tr16_b64_v4i16((__attribute__((address_space(3))) v4i16_t*)p)); }
DI bf16x8 cat8(s16x4 a, s16x4 b){ return __builtin_bit_cast(bf16x8, __builtin_shufflevector(a,b,0,1,2,3,4,5,6,7)); }
DI float dpp_add(float v, const int ctrl_sel){
  int t;
  if(ctrl_sel==0) t=__builtin_amdgcn_mov_dpp(__float_as_int(v),0xB1,0xF,0xF,true);
  else if(ctrl_sel==1) t=__builtin_amdgcn_mov_dpp(__float_as_int(v),0x4E,0xF,0xF,true);
  else t=__builtin_amdgcn_mov_dpp(__float_as_int(v),0x141,0xF,0xF,true);
  return v+__int_as_float(t);
}
DI float lo16f(unsigned u){ return __uint_as_float(u<<16); }
DI float hi16f(unsigned u){ return __uint_as_float(u&0xffff0000u); }
DI float dot2bf(unsigned a, unsigned b, float c){
  return __builtin_amdgcn_fdot2_f32_bf16(__builtin_bit_cast(bf16x2,a),__builtin_bit_cast(bf16x2,b),c,false); }

constexpr int HP=1408;
constexpr int K1_WST=24576, K1_NST=4, K1_X=K1_WST*K1_NST, K1_LDS=K1_X+8*8192;

DI void gu_store(const f32x16& ag, const f32x16& au, u16* hp, bool zero){
  u32x4 o0,o1;
  #pragma unroll
  for(int i=0;i<8;i++){
    float hv[2];
    #pragma unroll
    for(int e=0;e<2;e++){ float g=ag[2*i+e]; float sg=g*__builtin_amdgcn_rcpf(1.0f+__builtin_amdgcn_exp2f(-1.4426950408889634f*g)); hv[e]=zero?0.f:sg*au[2*i+e]; }
    unsigned w=pk2(hv[0],hv[1]); if(i<4) o0[i]=w; else o1[i-4]=w;
  }
  *(u32x4*)hp=o0; *(u32x4*)(hp+16)=o1;
}

__global__ __launch_bounds__(512,2) void k1_qkv(const u16* __restrict__ x, const u16* __restrict__ wqkv,
     u16* __restrict__ Qs, u16* __restrict__ Ks, u16* __restrict__ Vt,
     const u16* __restrict__ wg, const u16* __restrict__ wu, u16* __restrict__ hb, unsigned* __restrict__ ctl){
  extern __shared__ __attribute__((aligned(16))) char lds[];
  const int tid=threadIdx.x, lane=tid&63; const int wid=__builtin_amdgcn_readfirstlane(tid>>6);
  const int b=blockIdx.x;
  const int rb=(b&7)*4+((b>>3)&3), hg=b>>5;
  const unsigned lds0=(unsigned)(uintptr_t)lds;
  const int rr=lane>>3, p=lane&7;
  const int lrow=wid*8+rr;
  const u16* xp=x+(long)(rb*64+lrow)*D+p*8;
  u32x4 xv[8];
  #pragma unroll
  for(int i=0;i<8;i++) asm volatile("global_load_dwordx4 %0, %1, off offset:%2" : "=v"(xv[i]) : "v"(xp), "n"(i*128) : "memory");
  const u16* wp=wqkv+(long)lrow*(3*D)+hg*64+(((p>>1)^(((lrow>>1)&1)|(((lrow>>3)&1)<<1)))*16)+(p&1)*8;
  asm volatile("s_barrier":::"memory");
  #pragma unroll
  for(int st=0;st<K1_NST;st++){
    #pragma unroll
    for(int ty=0;ty<3;ty++) glds16(wp+(long)st*64*(3*D)+ty*D, lds0+st*K1_WST+ty*8192+wid*1024);
  }
  const int ft0=hg*5+(hg<3?hg:3);
  int goff[3];
  #pragma unroll
  for(int i=0;i<3;i++){
    const int pc=wid*3+i; const int i16=(pc%12)*64+lane; const int k=i16/24, ch=i16%24;
    int col=ft0*32+(((ch>>2)^((k>>1)&1))*32)+(ch&3)*8; col=col>F-8?F-8:col;
    goff[i]=k*F+col;
  }
  const int rot=GROT;
  const u16* gbase=(wid<4)?wg:wu;
#define GU_ISS(g_) do{ _Pragma("unroll") for(int i=0;i<3;i++) glds16(gbase+(long)(((g_)+rot)&15)*32*F+goff[i], lds0+((g_)&3)*K1_WST+(wid*3+i)*1024); }while(0)
  asm volatile("s_waitcnt vmcnt(12)" : "+v"(xv[0]),"+v"(xv[1]),"+v"(xv[2]),"+v"(xv[3]),"+v"(xv[4]),"+v"(xv[5]),"+v"(xv[6]),"+v"(xv[7]) :: "memory");
  float s1=0.f, s2=0.f;
  #pragma unroll
  for(int i=0;i<8;i++){
    #pragma unroll
    for(int j=0;j<4;j++){ s1=dot2bf(xv[i][j],0x3F803F80u,s1); s2=dot2bf(xv[i][j],xv[i][j],s2); }
  }
  s1=dpp_add(s1,0); s2=dpp_add(s2,0); s1=dpp_add(s1,1); s2=dpp_add(s2,1); s1=dpp_add(s1,2); s2=dpp_add(s2,2);
  const float mean=s1*(1.0f/D);
  float var=s2*(1.0f/D)-mean*mean;
  if(__builtin_expect(__any(!(mean*mean<=16.f*var)),0)){
    float q2=0.f;
    #pragma unroll
    for(int i=0;i<8;i++){
      #pragma unroll
      for(int j=0;j<4;j++){ float a=lo16f(xv[i][j])-mean, c=hi16f(xv[i][j])-mean; q2=fmaf(a,a,q2); q2=fmaf(c,c,q2); }
    }
    q2=dpp_add(q2,0); q2=dpp_add(q2,1); q2=dpp_add(q2,2);
    var=q2*(1.0f/D);
  }
  const float mb=rbf(mean);
  const float den=rbf(sqrtf(rbf(rbf(var)+1e-5f)));
  const float rinv=1.0f/den;
  char* xw=lds+K1_X+lrow*128+((p^((lrow>>1)&7))*16);
#define NRM1(i,j) do{ unsigned c_=pk2(lo16f(xv[i][j])-mb, hi16f(xv[i][j])-mb); xv[i][j]=pk2(lo16f(c_)*rinv, hi16f(c_)*rinv); }while(0)
#define NORM(i) do{ NRM1(i,0); NRM1(i,1); NRM1(i,2); NRM1(i,3); *(u32x4*)(xw+(i)*8192)=xv[i]; }while(0)
  NORM(0); NORM(1);
  const int rt2=wid&1, hd=wid>>1;
  const int g=lane>>4, r16=lane&15;
  const int xsw=(r16>>1)&7;
  lds_cptr xa0=(lds_cptr)lds+K1_X+(rt2*32+r16)*128+((g^xsw)*16);
  lds_cptr xa1=(lds_cptr)lds+K1_X+(rt2*32+r16)*128+(((4+g)^xsw)*16);
  const int q4=(lane&15)>>2, p4=lane&3;
  const int fk=((q4>>1)&1)|((g&1)<<1);
  lds_cptr wa=(lds_cptr)lds+(8*g+q4)*128+((hd^fk)*32)+p4*8;
  f32x4 aq[2]={}, ak[2]={}, av[2]={};
  bf16x8 fx[2][2][2], fw[2][2][3];
#define TRW(set,s,ty,c) fw[set][s][ty]=cat8(lds_tr(wa+((c)%K1_NST)*K1_WST+(ty)*8192+(s)*4096), lds_tr(wa+((c)%K1_NST)*K1_WST+(ty)*8192+(s)*4096+512))
  asm volatile("s_waitcnt vmcnt(9) lgkmcnt(0)\n\ts_barrier":::"memory");
  fx[0][0][0]=lds_ld(xa0); fx[0][0][1]=lds_ld(xa0+2048); fx[0][1][0]=lds_ld(xa1); fx[0][1][1]=lds_ld(xa1+2048);
  TRW(0,0,0,0); TRW(0,0,1,0); TRW(0,0,2,0); TRW(0,1,0,0); TRW(0,1,1,0); TRW(0,1,2,0);
#define K1_ISS1(c,i) do{ if((c)<4) glds16(wp+(long)((c)+4)*64*(3*D)+(i)*D, lds0+(c)*K1_WST+(i)*8192+wid*1024); \
                        else if((c)<7) glds16(gbase+(long)(((c)-4+rot)&15)*32*F+goff[i], lds0+((c)-4)*K1_WST+(wid*3+(i))*1024); }while(0)
  #pragma unroll
  for(int c=0;c<8;c++){
    const int cs=c&1, ns=cs^1; const int xb=(c+1)*8192; const int ni=(c+2)&7;
    if(c<7) asm volatile("s_waitcnt vmcnt(6) lgkmcnt(0)\n\ts_barrier":::"memory");
    aq[0]=MFMA16(fw[cs][0][0],fx[cs][0][0],aq[0]); if(c<7){ fx[ns][0][0]=lds_ld(xa0+xb); fx[ns][0][1]=lds_ld(xa0+xb+2048); } SB();
    aq[1]=MFMA16(fw[cs][0][0],fx[cs][0][1],aq[1]); if(c<7){ TRW(ns,0,0,c+1); } SB();
    ak[0]=MFMA16(fw[cs][0][1],fx[cs][0][0],ak[0]); if(c<6) NRM1(ni,0); SB();
    ak[1]=MFMA16(fw[cs][0][1],fx[cs][0][1],ak[1]); if(c<7){ TRW(ns,0,1,c+1); } SB();
    av[0]=MFMA16(fx[cs][0][0],fw[cs][0][2],av[0]); if(c<7){ TRW(ns,0,2,c+1); } K1_ISS1(c,0); SB();
    av[1]=MFMA16(fx[cs][0][1],fw[cs][0][2],av[1]); if(c<6) NRM1(ni,1); SB();
    aq[0]=MFMA16(fw[cs][1][0],fx[cs][1][0],aq[0]); if(c<7){ fx[ns][1][0]=lds_ld(xa1+xb); fx[ns][1][1]=lds_ld(xa1+xb+2048); } SB();
    aq[1]=MFMA16(fw[cs][1][0],fx[cs][1][1],aq[1]); if(c<7){ TRW(ns,1,0,c+1); } K1_ISS1(c,1); SB();
    ak[0]=MFMA16(fw[cs][1][1],fx[cs][1][0],ak[0]); if(c<6) NRM1(ni,2); SB();
    ak[1]=MFMA16(fw[cs][1][1],fx[cs][1][1],ak[1]); if(c<7){ TRW(ns,1,1,c+1); } SB();
    av[0]=MFMA16(fx[cs][1][0],fw[cs][1][2],av[0]); if(c<7){ TRW(ns,1,2,c+1); } K1_ISS1(c,2); SB();
    av[1]=MFMA16(fx[cs][1][1],fw[cs][1][2],av[1]); if(c<6){ NRM1(ni,3); *(u32x4*)(xw+ni*8192)=xv[ni]; } SB();
  }
  {
    const int h=hg*4+hd; const int T=rb*2+rt2;
    #pragma unroll
    for(int tt=0;tt<2;tt++){
      const long qo=(long)h*S*DH+T*512+(g>>1)*256+(tt*16+r16)*8+(g&1)*4;
      u32x2 wq={pk2(aq[tt][0]*QSCALE,aq[tt][1]*QSCALE),pk2(aq[tt][2]*QSCALE,aq[tt][3]*QSCALE)};
      u32x2 wk={pk2(ak[tt][0],ak[tt][1]),pk2(ak[tt][2],ak[tt][3])};
      const long vo=(long)h*S*DH+(2*T+tt)*256+(g&1)*128+r16*8+(g>>1)*4;
      u32x2 wv={pk2(av[tt][0],av[tt][1]),pk2(av[tt][2],av[tt][3])};
      asm volatile("global_store_dwordx2 %0, %1, off\n\tglobal_store_dwordx2 %2, %3, off\n\tglobal_store_dwordx2 %4, %5, off"
         :: "v"(Qs+qo),"v"(wq),"v"(Ks+qo),"v"(wk),"v"(Vt+vo),"v"(wv) : "memory");
    }
  }
  {
    const int r=lane&31, hh=lane>>5;
    const int ftl = wid<4 ? wid : 4+((wid-4)>>1);
    const int tt0 = wid<4 ? 0 : ((wid-4)&1);
    const int xsw2=(r>>1)&7;
    lds_cptr xg=(lds_cptr)lds+K1_X+(tt0*32+r)*128;
    const int blk=(lane>>4)&1;
    lds_cptr wg_=(lds_cptr)lds+(8*hh+q4)*384+((ftl^((q4>>1)&1))*64)+blk*32+p4*8;
    f32x16 ag[2]={}, au[2]={};
    bf16x8 gf[2][2], uf[2][2], xf[2][2][2];
    const bool two=(wid<4);
#define GRD(gs_,s) do{ const int st_=(gs_)&1; lds_cptr w_=wg_+((gs_)&3)*K1_WST+(s)*6144; \
      gf[st_][s]=cat8(lds_tr(w_), lds_tr(w_+1536)); uf[st_][s]=cat8(lds_tr(w_+12288), lds_tr(w_+12288+1536)); \
      const int gr_=((gs_)+rot)&15; lds_cptr x_=xg+(gr_>>1)*8192+(((4*(gr_&1)+2*(s)+hh)^xsw2)*16); xf[st_][s][0]=lds_ld(x_); if(two) xf[st_][s][1]=lds_ld(x_+4096); }while(0)
#define GU_ISS1(g_,i) glds16(gbase+(long)(((g_)+rot)&15)*32*F+goff[i], lds0+((g_)&3)*K1_WST+(wid*3+(i))*1024)
    asm volatile("s_waitcnt vmcnt(12) lgkmcnt(0)\n\ts_barrier":::"memory");
    GRD(0,0); GRD(0,1);
    GU_ISS(3);
#define GWAIT(gs) do{ \
      if((gs)<2) asm volatile("s_waitcnt vmcnt(12) lgkmcnt(0)\n\ts_barrier":::"memory"); \
      else if((gs)<13) asm volatile("s_waitcnt vmcnt(6) lgkmcnt(0)\n\ts_barrier":::"memory"); \
      else if((gs)==13) asm volatile("s_waitcnt vmcnt(3) lgkmcnt(0)\n\ts_barrier":::"memory"); \
      else if((gs)==14) asm volatile("s_waitcnt vmcnt(0) lgkmcnt(0)\n\ts_barrier":::"memory"); }while(0)
#define RG(gn,s) do{ if((gn)<16){ lds_cptr w_=wg_+((gn)&3)*K1_WST+(s)*6144; gf[(gn)&1][s]=cat8(lds_tr(w_), lds_tr(w_+1536)); } }while(0)
#define RU(gn,s) do{ if((gn)<16){ lds_cptr w_=wg_+((gn)&3)*K1_WST+(s)*6144+12288; uf[(gn)&1][s]=cat8(lds_tr(w_), lds_tr(w_+1536)); } }while(0)
#define RX(gn,s,t) do{ if((gn)<16){ const int gr_=((gn)+rot)&15; xf[(gn)&1][s][t]=lds_ld(xg+(gr_>>1)*8192+(((4*(gr_&1)+2*(s)+hh)^xsw2)*16)+(t)*4096); } }while(0)
#define MG(gs,s,t) ag[t]=MFMA32(gf[(gs)&1][s],xf[(gs)&1][s][t],ag[t])
#define MU(gs,s,t) au[t]=MFMA32(uf[(gs)&1][s],xf[(gs)&1][s][t],au[t])
#define GI(gs,i) do{ if((gs)+4<16) GU_ISS1((gs)+4,i); }while(0)
    if(two){
      #pragma unroll
      for(int gs=0;gs<16;gs++){
        GWAIT(gs);
        MG(gs,0,0); RG(gs+1,0); SB();
        MU(gs,0,0); RU(gs+1,0); SB();
        MG(gs,0,1); RX(gs+1,0,0); RX(gs+1,0,1); SB();
        MU(gs,0,1); RG(gs+1,1); SB();
        MG(gs,1,0); RU(gs+1,1); SB();
        MU(gs,1,0); RX(gs+1,1,0); RX(gs+1,1,1); SB();
        MG(gs,1,1); GI(gs,0); GI(gs,1); SB();
        MU(gs,1,1); GI(gs,2); SB();
      }
    } else if((ftl<5)||(hg<3)) {
      #pragma unroll
      for(int gs=0;gs<16;gs++){
        GWAIT(gs);
        GI(gs,0); SB();
        MG(gs,0,0); RG(gs+1,0); RU(gs+1,0); SB();
        MU(gs,0,0); RX(gs+1,0,0); RG(gs+1,1); SB();
        MG(gs,1,0); RU(gs+1,1); RX(gs+1,1,0); SB();
        MU(gs,1,0); GI(gs,1); GI(gs,2); SB();
      }
    } else {
      #pragma unroll
      for(int gs=0;gs<16;gs++){ GWAIT(gs); GI(gs,0); GI(gs,1); GI(gs,2); }
    }
    const int ft=ft0+ftl;
    const bool own=(ftl<5)||(hg<3)||(hg==7);
    if(own){
      u16* hp=hb+(long)(rb*64+tt0*32+r)*HP+ft*32+hh*8;
      gu_store(ag[0],au[0],hp,ft>=43);
      if(wid<4) gu_store(ag[1],au[1],hp+(long)32*HP,false);
    }
  }
  if(b==0){ ctl[tid]=0u; ctl[512+tid]=0u; }
}

#define GU_PRIO 2
constexpr int A2_K=0, A2_V=32768, A2_C=65536, K2_LDS=65536+4096+16;
#define EX(x) __builtin_amdgcn_exp2f(x)
DI void pv_step(const f32x16& sc, bf16x8 v0, bf16x8 v1, f32x16& o){
  u32x4 p0, p1;
  #pragma unroll
  for(int i=0;i<4;i++){ p0[i]=pk2(EX(sc[2*i]),EX(sc[2*i+1])); }
  #pragma unroll
  for(int i=0;i<4;i++){ p1[i]=pk2(EX(sc[8+2*i]),EX(sc[8+2*i+1])); }
  o=MFMA32(v0,__builtin_bit_cast(bf16x8,p0),o);
  o=MFMA32(v1,__builtin_bit_cast(bf16x8,p1),o);
}
DI void attn_role(char* lds, const u16* __restrict__ Qs, const u16* __restrict__ Ks, const u16* __restrict__ Vt, u16* O, int b, unsigned* ctl){
  const int tid=threadIdx.x, lane=tid&63; const int wid=__builtin_amdgcn_readfirstlane(tid>>6);
  const int h=(b&7)*4+((b>>3)>>3), qb=(b>>3)&7;
  const int r=lane&31, hh=lane>>5;
  const unsigned lds0=(unsigned)(uintptr_t)lds;
  const u16* qp=Qs+(long)h*S*DH+(qb*8+wid)*512+lane*8;
  bf16x8 qf;
  asm volatile("global_load_dwordx4 %0, %1, off" : "=v"(qf) : "v"(qp) : "memory");
  const u16* kg=Ks+(long)h*S*DH+wid*512+lane*8;
  const u16* vg=Vt+(long)h*S*DH+wid*512+lane*8;
#define A_ISSUE(c) do{ glds16(kg+(c)*4096, lds0+A2_K+((c)&3)*8192+wid*1024); glds16(vg+(c)*4096, lds0+A2_V+((c)&3)*8192+wid*1024); }while(0)
  A_ISSUE(0); A_ISSUE(1); A_ISSUE(2); A_ISSUE(3);
  { u32x2 v; v[0]=((tid&31)<2)?0x3F803F80u:0u; v[1]=v[0]; *(u32x2*)(lds+A2_C+tid*8)=v; }
  lds_cptr kp=(lds_cptr)lds+A2_K+lane*16;
  const bool cl=(r>=16);
  lds_cptr vp=(lds_cptr)lds+(cl ? A2_C : A2_V)+hh*256+(r&15)*16;
  const int vinc= cl?0:1024; const int vwrap= cl?0:(1024-32768);
  asm volatile("s_waitcnt vmcnt(6) lgkmcnt(0)\n\ts_barrier": "+v"(qf) ::"memory");
  f32x16 negm; f32x16 sA, sB;
  bf16x8 kA, kB, vA0, vA1, vB0, vB1;
  {
    f32x16 z={}; bf16x8 k0=lds_ld(kp);
    kA=lds_ld(kp+1024); vA0=lds_ld(vp); vA1=lds_ld(vp+512); vp+=vinc;
    f32x16 s0=MFMA32(k0,qf,z);
    float m=s0[0];
    #pragma unroll
    for(int i=1;i<16;i++) m=fmaxf(m,s0[i]);
    #pragma unroll
    for(int t=1;t<4;t++){
      f32x16 st=MFMA32(t==1?kA:lds_ld(kp+t*1024),qf,z);
      #pragma unroll
      for(int i=0;i<16;i++) m=fmaxf(m,st[i]);
    }
    auto rr=__builtin_amdgcn_permlane32_swap(__float_as_uint(m),__float_as_uint(m),false,false);
    m=fmaxf(__uint_as_float(rr[0]),__uint_as_float(rr[1]));
    float nm=-(m+80.f);
    #pragma unroll
    for(int i=0;i<16;i++){ negm[i]=nm; sA[i]=s0[i]+nm; }
  }
  f32x16 o={};
#define STEP(sC,sN,kU,kL,vU0,vU1,vL0,vL1,t) do{ \
    u32x4 p0,p1; \
    kL=lds_ld(kp+(((t)+2)&31)*1024); vL0=lds_ld(vp); vL1=lds_ld(vp+512); vp+=((((t)+1)&31)==31)?vwrap:vinc; \
    float e0=EX(sC[0]),e1=EX(sC[1]),e2=EX(sC[2]),e3=EX(sC[3]); SB(); \
    sN=MFMA32(kU,qf,negm); SB(); \
    float e4=EX(sC[4]),e5=EX(sC[5]),e6=EX(sC[6]),e7=EX(sC[7]); \
    p0[0]=pk2(e0,e1); p0[1]=pk2(e2,e3); p0[2]=pk2(e4,e5); p0[3]=pk2(e6,e7); SB(); \
    float e8=EX(sC[8]),e9=EX(sC[9]); SB(); \
    o=MFMA32(vU0,__builtin_bit_cast(bf16x8,p0),o); SB(); \
    float e10=EX(sC[10]),e11=EX(sC[11]),e12=EX(sC[12]),e13=EX(sC[13]),e14=EX(sC[14]),e15=EX(sC[15]); \
    p1[0]=pk2(e8,e9); p1[1]=pk2(e10,e11); p1[2]=pk2(e12,e13); p1[3]=pk2(e14,e15); SB(); \
    o=MFMA32(vU1,__builtin_bit_cast(bf16x8,p1),o); SB(); \
  }while(0)
#define ITER(t) do{ STEP(sA,sB,kA,kB,vA0,vA1,vB0,vB1,(t)); STEP(sB,sA,kB,kA,vB0,vB1,vA0,vA1,(t)+1); }while(0)
  #pragma unroll
  for(int j=0;j<8;j++){
    ITER(8*j); ITER(8*j+2); ITER(8*j+4);
    if(j<7){
      if(j==0) asm volatile("s_waitcnt vmcnt(4) lgkmcnt(0)\n\ts_barrier":::"memory");
      else if(j<6) asm volatile("s_waitcnt vmcnt(2) lgkmcnt(0)\n\ts_barrier":::"memory");
      else asm volatile("s_waitcnt vmcnt(0) lgkmcnt(0)\n\ts_barrier":::"memory");
      if(j>=1 && j+3<8) A_ISSUE(j+3);
    }
    ITER(8*j+6);
  }
  float l=o[8];
  { auto rr=__builtin_amdgcn_permlane32_swap(__float_as_uint(l),__float_as_uint(l),false,false); l=__uint_as_float(rr[0]); }
  bool bad = !(l<1e36f);
  #pragma unroll
  for(int i=0;i<8;i++) bad = bad || !(fabsf(o[i])<3e38f);
  if(__builtin_expect(__any(bad),0)){
    const u16* kgl=Ks+(long)h*S*DH+lane*8;
    const u16* vgl=Vt+(long)h*S*DH+hh*128+(r&15)*8;
    float m=-INFINITY; f32x16 z={};
    #pragma unroll
    for(int i=0;i<16;i++) o[i]=0.f;
    for(int t=0;t<64;t++){
      bf16x8 kf=*(const bf16x8*)(kgl+t*512);
      bf16x8 v0=*(const bf16x8*)(vgl+t*512), v1=*(const bf16x8*)(vgl+t*512+256);
      if(cl){ u32x4 one={0x3F803F80u,0x3F803F80u,0x3F803F80u,0x3F803F80u}; v0=__builtin_bit_cast(bf16x8,one); v1=v0; }
      f32x16 s0=MFMA32(kf,qf,z);
      float tm=s0[0];
      #pragma unroll
      for(int i=1;i<16;i++) tm=fmaxf(tm,s0[i]);
      auto rr=__builtin_amdgcn_permlane32_swap(__float_as_uint(tm),__float_as_uint(tm),false,false);
      tm=fmaxf(__uint_as_float(rr[0]),__uint_as_float(rr[1]));
      float mn=fmaxf(m,tm);
      float alpha=EX(m-mn); m=mn;
      #pragma unroll
      for(int i=0;i<16;i++){ o[i]*=alpha; s0[i]-=mn; }
      pv_step(s0,v0,v1,o);
    }
    l=o[8];
    auto rr=__builtin_amdgcn_permlane32_swap(__float_as_uint(l),__float_as_uint(l),false,false); l=__uint_as_float(rr[0]);
  }
  float rl=1.0f/l;
  u32x4 w={pk2(o[0]*rl,o[1]*rl),pk2(o[2]*rl,o[3]*rl),pk2(o[4]*rl,o[5]*rl),pk2(o[6]*rl,o[7]*rl)};
  asm volatile("global_store_dwordx4 %0, %1, off sc1\n\ts_waitcnt vmcnt(0)\n\ts_barrier" :: "v"(O+(long)((qb*8+wid)*32+r)*D+h*DH+hh*8), "v"(w) : "memory");
  if(tid==0) __hip_atomic_fetch_add(ctl+qb*32,1u,__ATOMIC_RELAXED,__HIP_MEMORY_SCOPE_AGENT);
}
DI void down_role(char* lds, const u16* __restrict__ x, const u16* __restrict__ hb, const u16* __restrict__ wd, const u16* ob, const u16* __restrict__ wo, u16* __restrict__ out, unsigned* ctl, int mt, int nt){
  const int tid=threadIdx.x, lane=tid&63; const int wid=__builtin_amdgcn_readfirstlane(tid>>6);
  const unsigned lds0=(unsigned)(uintptr_t)lds;
  __builtin_amdgcn_s_setprio(GU_PRIO);
  for(int i=0;i<GU_DELAY;i++) __builtin_amdgcn_s_sleep(16);
  const int rr=lane>>3, sl=lane&7;
  const int prow=wid*8+rr;
  const u16* hs=hb+(long)(mt*64+prow)*HP+((sl^((prow>>1)&7))*8);
  const int wc=nt*64+(((sl>>2)^((prow>>1)&1))*32)+(sl&3)*8;
  auto issue=[&](int c){ const unsigned b_=lds0+(c&3)*16384+wid*1024; int k=c*64+prow; k=k>F-1?F-1:k;
    glds16(hs+c*64,b_); glds16(wd+(long)k*D+wc,b_+8192); };
  issue(0); issue(1); issue(2);
  const int wm=wid&1, wn=(wid>>1)&1, kh=wid>>2; const int r=lane&31, hh=lane>>5;
  const int xr=wm*32+r; const int xsw=(xr>>1)&7;
  lds_cptr xa0=(lds_cptr)lds+xr*128+(((4*kh+hh)^xsw)*16);
  lds_cptr xa1=(lds_cptr)lds+xr*128+(((4*kh+2+hh)^xsw)*16);
  const int blk=(lane>>4)&1, q4=(lane&15)>>2, p4=lane&3;
  lds_cptr wa=(lds_cptr)lds+8192+(32*kh+4*hh+q4)*128+((wn^((q4>>1)&1))*64)+blk*32+p4*8;
  f32x16 acc={};
  #pragma unroll 1
  for(int c=0;c<22;c++){
    if(c<20) asm volatile("s_waitcnt vmcnt(4) lgkmcnt(0)\n\ts_barrier":::"memory");
    else asm volatile("s_waitcnt vmcnt(0) lgkmcnt(0)\n\ts_barrier":::"memory");
    const int sb=(c&3)*16384;
    bf16x8 x0=lds_ld(xa0+sb), x1=lds_ld(xa1+sb);
    bf16x8 w0=cat8(lds_tr(wa+sb), lds_tr(wa+sb+1024));
    bf16x8 w1=cat8(lds_tr(wa+sb+2048), lds_tr(wa+sb+2048+1024));
    acc=MFMA32(w0,x0,acc); acc=MFMA32(w1,x1,acc);
    SB();
    if(c+3<22) issue(c+3);
  }
  asm volatile("s_waitcnt lgkmcnt(0)\n\ts_barrier":::"memory");
  { const u16* ws_=wo+(long)prow*D+wc;
    #pragma unroll
    for(int c=0;c<8;c++) glds16(ws_+(long)c*64*D, lds0+c*8192+wid*1024); }
  const long ro=(long)(mt*64+xr)*D+nt*64+wn*32+4*hh;
  asm volatile("s_waitcnt vmcnt(0)\n\ts_barrier":::"memory");
  bf16x8 wf[16];
  { lds_cptr wb=wa-8192;
    #pragma unroll
    for(int c=0;c<8;c++){
      wf[2*c]=cat8(lds_tr(wb+c*8192), lds_tr(wb+c*8192+1024));
      wf[2*c+1]=cat8(lds_tr(wb+c*8192+2048), lds_tr(wb+c*8192+2048+1024));
    } }
  asm volatile("s_waitcnt lgkmcnt(0)" : "+v"(wf[0]),"+v"(wf[1]),"+v"(wf[2]),"+v"(wf[3]),"+v"(wf[4]),"+v"(wf[5]),"+v"(wf[6]),"+v"(wf[7]),
      "+v"(wf[8]),"+v"(wf[9]),"+v"(wf[10]),"+v"(wf[11]),"+v"(wf[12]),"+v"(wf[13]),"+v"(wf[14]),"+v"(wf[15]) :: "memory");
  __builtin_amdgcn_s_setprio(0);
  if(tid==0){
    unsigned* cnt=ctl+(mt>>2)*32; unsigned spins=0;
    while(__hip_atomic_load(cnt,__ATOMIC_RELAXED,__HIP_MEMORY_SCOPE_AGENT)<32u){ __builtin_amdgcn_s_sleep(POLL_SLEEP); if(++spins>(1u<<21)) break; }
  }
  asm volatile("s_barrier":::"memory");
  {
    const u16* os=ob+(long)(mt*64+prow)*D+sl*8;
    char* ow=lds+prow*128+((sl^((prow>>1)&7))*16);
    u32x4 t0,t1,t2,t3,t4,t5,t6,t7;
    asm volatile("global_load_dwordx4 %0, %8, off sc1\n\tglobal_load_dwordx4 %1, %8, off offset:128 sc1\n\tglobal_load_dwordx4 %2, %8, off offset:256 sc1\n\tglobal_load_dwordx4 %3, %8, off offset:384 sc1\n\t"
                 "global_load_dwordx4 %4, %8, off offset:512 sc1\n\tglobal_load_dwordx4 %5, %8, off offset:640 sc1\n\tglobal_load_dwordx4 %6, %8, off offset:768 sc1\n\tglobal_load_dwordx4 %7, %8, off offset:896 sc1\n\ts_waitcnt vmcnt(0)"
      : "=&v"(t0),"=&v"(t1),"=&v"(t2),"=&v"(t3),"=&v"(t4),"=&v"(t5),"=&v"(t6),"=&v"(t7) : "v"(os) : "memory");
    *(u32x4*)(ow)=t0; *(u32x4*)(ow+8192)=t1; *(u32x4*)(ow+2*8192)=t2; *(u32x4*)(ow+3*8192)=t3;
    *(u32x4*)(ow+4*8192)=t4; *(u32x4*)(ow+5*8192)=t5; *(u32x4*)(ow+6*8192)=t6; *(u32x4*)(ow+7*8192)=t7;
  }
  u32x2 xr4[4];
  #pragma unroll
  for(int q=0;q<4;q++) xr4[q]=*(const u32x2*)(x+ro+8*q);
  asm volatile("s_waitcnt lgkmcnt(0)\n\ts_barrier":::"memory");
  #pragma unroll
  for(int c=0;c<8;c++){
    bf16x8 x0=lds_ld(xa0+c*8192), x1=lds_ld(xa1+c*8192);
    acc=MFMA32(wf[2*c],x0,acc); acc=MFMA32(wf[2*c+1],x1,acc);
  }
  asm volatile("s_waitcnt lgkmcnt(0)\n\ts_barrier":::"memory");
  float* xch=(float*)lds+(wid&3)*1024+lane;
  if(kh==1){
    #pragma unroll
    for(int i=0;i<16;i++) xch[i*64]=acc[i];
  }
  __syncthreads();
  if(kh==1) return;
  #pragma unroll
  for(int q=0;q<4;q++){
    float v0=acc[4*q]+xch[(4*q)*64]+lo16f(xr4[q][0]), v1=acc[4*q+1]+xch[(4*q+1)*64]+hi16f(xr4[q][0]);
    float v2=acc[4*q+2]+xch[(4*q+2)*64]+lo16f(xr4[q][1]), v3=acc[4*q+3]+xch[(4*q+3)*64]+hi16f(xr4[q][1]);
    u32x2 w={pk2(v0,v1),pk2(v2,v3)};
    *(u32x2*)(out+ro+8*q)=w;
  }
}
__global__ __launch_bounds__(512,4) void k2_fused(const u16* __restrict__ Qs, const u16* __restrict__ Ks, const u16* __restrict__ Vt, u16* O,
     const u16* __restrict__ hb, const u16* __restrict__ x, const u16* __restrict__ wd, const u16* __restrict__ wo, u16* __restrict__ out, unsigned* ctl){
  extern __shared__ __attribute__((aligned(16))) char lds[];
  const int b=blockIdx.x;
  int task;
  {
    int* sh=(int*)(lds+K2_LDS-16);
    if(threadIdx.x<64){
      const int ln=threadIdx.x;
      const unsigned tg=TGSEL;
      unsigned* claim=ctl+512;
      int t=(int)tg*256+(b&255);
      unsigned old=0;
      if(ln==0) old=__hip_atomic_fetch_add(claim+t,1u,__ATOMIC_RELAXED,__HIP_MEMORY_SCOPE_AGENT);
      old=(unsigned)__builtin_amdgcn_readfirstlane((int)old);
      if(old!=0u){
        t=-1;
        for(int blk=0; blk<8 && t<0; ){
          const int c=((blk<4)?(int)tg*256:256-(int)tg*256)+(blk&3)*64+ln;
          const unsigned v=__hip_atomic_load(claim+c,__ATOMIC_RELAXED,__HIP_MEMORY_SCOPE_AGENT);
          const unsigned long long fr=__ballot(v==0u);
          if(fr==0ull){ blk++; continue; }
          const int rot=b&63; const unsigned long long f2=(fr>>rot)|(rot?(fr<<(64-rot)):0ull);
          const int pick=(__builtin_ctzll(f2)+rot)&63;
          unsigned o2=1;
          if(ln==pick) o2=__hip_atomic_fetch_add(claim+c,1u,__ATOMIC_RELAXED,__HIP_MEMORY_SCOPE_AGENT);
          o2=(unsigned)__builtin_amdgcn_readlane((int)o2,pick);
          if(o2==0u) t=c-ln+pick;
        }
      }
      if(ln==0) *sh=t;
    }
    __syncthreads();
    task=__builtin_amdgcn_readfirstlane(*sh);
    __syncthreads();
  }
  if(task<0) return;
  if(task<256) attn_role(lds,Qs,Ks,Vt,O,task,ctl);
  else { const int u=task-256; down_role(lds,x,hb,wd,O,wo,out,ctl,(u&7)*4+((u>>3)&3),u>>5); }
}

extern "C" void kernel_launch(void* const* d_in, const int* in_sizes, int n_in, void* d_out, int out_size, void* d_ws, size_t ws_size, hipStream_t stream){
  const u16* x=(const u16*)d_in[0]; const u16* wqkv=(const u16*)d_in[1]; const u16* wo=(const u16*)d_in[2];
  const u16* wg=(const u16*)d_in[4]; const u16* wu=(const u16*)d_in[5]; const u16* wd=(const u16*)d_in[6];
  char* ws=(char*)d_ws;
  unsigned* ctl=(unsigned*)ws; ws+=4096;
  u16* hb=(u16*)ws; ws+=(size_t)S*HP*2;
  u16* o=(u16*)ws; ws+=(size_t)S*D*2;
  u16* Qs=(u16*)ws; ws+=(size_t)S*D*2;
  u16* Ks=(u16*)ws; ws+=(size_t)S*D*2;
  u16* Vt=(u16*)ws; ws+=(size_t)S*D*2;
  (void)hipFuncSetAttribute((const void*)k2_fused, hipFuncAttributeMaxDynamicSharedMemorySize, K2_LDS);
  (void)hipFuncSetAttribute((const void*)k1_qkv, hipFuncAttributeMaxDynamicSharedMemorySize, K1_LDS);
  k1_qkv<<<256,512,K1_LDS,stream>>>(x,wqkv,Qs,Ks,Vt,wg,wu,hb,ctl);
  k2_fused<<<512,512,K2_LDS,stream>>>(Qs,Ks,Vt,o,hb,x,wd,wo,(u16*)d_out,ctl);
}
```
